```python
import torch

def _softmax(x, axis=-1):
    return torch.softmax(x, dim=axis)
def _gelu(x):
    return torch.nn.functional.gelu(x, approximate="tanh")
def _silu(x):
    return torch.nn.functional.silu(x)
def _sigmoid(x):
    return torch.sigmoid(x)
def _layernorm(x, axis=-1, eps=1e-5):
    m = x.mean(dim=axis, keepdim=True)
    v = x.var(dim=axis, keepdim=True, unbiased=False)
    return (x - m) / torch.sqrt(v + eps)
def _scatter_add(tgt, idx, src, axis=0):
    return tgt.clone().index_add_(axis, idx, src)
def _segment_sum(src, seg, n):
    out = torch.zeros((n,) + src.shape[1:], dtype=src.dtype)
    return out.index_add_(0, seg, src)
def _pad(x, pw):
    flat = tuple(v for pair in reversed(pw) for v in pair)
    return torch.nn.functional.pad(x, flat)

kernel_name = 'parallel_sum__gqa_block_cached__swiglu_ffn__decode'

def setup_inputs():

    _w = (lambda a: (lambda t: t.to(torch.bfloat16) if t.is_floating_point() else t)(torch.as_tensor(a)))
    def _A_setup():
        B, Sq, D = 1, 1, 448
        Hq, Hkv, Sk, Dh = 16, 8, 32768, 256
        assert Hq % Hkv == 0, (Hq, Hkv)
        return {
            "x": _w((lambda *s: torch.randn(*s, dtype=torch.bfloat16) * 0.05)(B, Sq, D)),
            "k_cache": _w((lambda *s: torch.randn(*s, dtype=torch.bfloat16) * 3.2)(B, Hkv, Sk, Dh)),
            "v_cache": _w((lambda *s: torch.randn(*s, dtype=torch.bfloat16) * 1.6)(B, Hkv, Sk, Dh)),
            "wq": _w((lambda *s: torch.randn(*s, dtype=torch.bfloat16) * 3.2)(D, Hq * Dh) * (D ** -0.5)),
            "wo": _w((lambda *s: torch.randn(*s, dtype=torch.bfloat16) * 1.6)(Hq * Dh, D) * ((Hq * Dh) ** -0.5)),
        }
    _A_in = _A_setup()

    def _B_setup():
        B, S, D, F = 1, 1, 448, 1792
        return {
            "x": _w((lambda *s: torch.randn(*s, dtype=torch.bfloat16) * 0.05)(B, S, D)),
            "w_gate": _w((lambda *s: torch.randn(*s, dtype=torch.bfloat16) * 0.05)(D, F)),
            "w_up": _w((lambda *s: torch.randn(*s, dtype=torch.bfloat16) * 0.05)(D, F)),
            "w_down": _w((lambda *s: torch.randn(*s, dtype=torch.bfloat16) * 0.05)(F, D)),
        }
    _B_in = _B_setup()

    return {
        "A_x": _A_in["x"],
        "A_k_cache": _A_in["k_cache"],
        "A_v_cache": _A_in["v_cache"],
        "A_wq": _A_in["wq"],
        "A_wo": _A_in["wo"],
        "B_x": _B_in["x"],
        "B_w_gate": _B_in["w_gate"],
        "B_w_up": _B_in["w_up"],
        "B_w_down": _B_in["w_down"],
    }

def reference(A_x, A_k_cache, A_v_cache, A_wq, A_wo, B_x, B_w_gate, B_w_up, B_w_down):
    def _A_ref(x, k_cache, v_cache, wq, wo):
        B, Sq, D = x.shape
        Hq, Dh = 16, 256
        Hkv = k_cache.shape[1]
        rep = Hq // Hkv
        q = (lambda x, *d: x.permute(*d))((x @ wq).reshape(B, Sq, Hq, Dh), 0, 2, 1, 3)
        k = (lambda x, n, axis: torch.repeat_interleave(x, n, dim=axis))(k_cache, rep, axis=1)
        v = (lambda x, n, axis: torch.repeat_interleave(x, n, dim=axis))(v_cache, rep, axis=1)
        scores = torch.einsum("bhqd,bhkd->bhqk", q, k) * (Dh ** -0.5)
        p = _softmax(scores, axis=-1)
        o = torch.einsum("bhqk,bhkd->bhqd", p, v)
        return (lambda x, *d: x.permute(*d))(o, 0, 2, 1, 3).reshape(B, Sq, Hq * Dh) @ wo

    def _B_ref(x, w_gate, w_up, w_down):
        g = _silu(x @ w_gate)
        u = x @ w_up
        return (g * u) @ w_down

    x = A_x
    xn = _layernorm(x, axis=-1)
    return x + _A_ref(xn, A_k_cache, A_v_cache, A_wq, A_wo) + _B_ref(xn, B_w_gate, B_w_up, B_w_down)
```

```cpp
#include <hip/hip_runtime.h>
#include <hip/hip_bf16.h>
#include <stdint.h>

#define MIMIC 0

typedef unsigned u32x4 __attribute__((ext_vector_type(4)));
typedef float f32x4 __attribute__((ext_vector_type(4)));
typedef __bf16 bf16x2 __attribute__((ext_vector_type(2)));

constexpr int D = 448, HQ = 16, HKV = 8, SK = 32768, DH = 256, FF = 1792;
constexpr int NSPLIT = 32;

__device__ __forceinline__ float bf2f(unsigned short b) { return __builtin_bit_cast(float, (unsigned)b << 16); }
__device__ __forceinline__ float rbf(float f) {
  __bf16 b = (__bf16)f; return (float)b;
}
__device__ __forceinline__ unsigned short f2bf(float f) { __bf16 b = (__bf16)f; return __builtin_bit_cast(unsigned short, b); }
__device__ __forceinline__ float dot2(unsigned k, unsigned q, float acc) {
  return __builtin_amdgcn_fdot2_f32_bf16(__builtin_bit_cast(bf16x2, k), __builtin_bit_cast(bf16x2, q), acc, false);
}

__device__ void layernorm_to_lds(const unsigned short* x, float* xn, float* red) {
  int t = threadIdx.x;
  float a = (t < D) ? bf2f(x[t]) : 0.f;
  float b = (t + 256 < D) ? bf2f(x[t + 256]) : 0.f;
  float s = a + b;
  for (int o = 32; o > 0; o >>= 1) s += __shfl_xor(s, o);
  if ((t & 63) == 0) red[t >> 6] = s;
  __syncthreads();
  float mean = (red[0] + red[1] + red[2] + red[3]) * (1.f / D);
  __syncthreads();
  float da = (t < D) ? a - mean : 0.f, db = (t + 256 < D) ? b - mean : 0.f;
  float v = da * da + db * db;
  for (int o = 32; o > 0; o >>= 1) v += __shfl_xor(v, o);
  if ((t & 63) == 0) red[t >> 6] = v;
  __syncthreads();
  float var = (red[0] + red[1] + red[2] + red[3]) * (1.f / D);
  float r = 1.f / sqrtf(var + 1e-5f);
  if (t < D) xn[t] = da * r;
  if (t + 256 < D) xn[t + 256] = db * r;
  __syncthreads();
}

__global__ void __launch_bounds__(256) k1(const unsigned short* x, const unsigned short* wq, const unsigned short* wg,
                                          const unsigned short* wu, unsigned short* qout, float* hout) {
  __shared__ float xn[D]; __shared__ float red[4];
  layernorm_to_lds(x, xn, red);
  int t = threadIdx.x, b = blockIdx.x;
  if (b < 16) {
    int c = b * 256 + t; float a = 0.f;
    for (int r = 0; r < D; ++r) a = fmaf(xn[r], bf2f(wq[(size_t)r * 4096 + c]), a);
    qout[c] = f2bf(a);
  } else {
    int c = (b - 16) * 256 + t; float g = 0.f, u = 0.f;
    for (int r = 0; r < D; ++r) { g = fmaf(xn[r], bf2f(wg[(size_t)r * FF + c]), g); u = fmaf(xn[r], bf2f(wu[(size_t)r * FF + c]), u); }
    hout[c] = g / (1.f + __expf(-g)) * u;
  }
}

__global__ void __launch_bounds__(512) k2(const unsigned short* K, const unsigned short* V, const unsigned short* q,
                                          float* pacc, float* pml) {
  __shared__ float sp[8][256];
  __shared__ float sacc[8][512];
  __shared__ float sml[8][4];
  const int lane = threadIdx.x & 63, wave = threadIdx.x >> 6;
  const int g = blockIdx.x & 7, s = blockIdx.x >> 3;
  const int half = lane >> 5, c = lane & 31;
  const u32x4* kp = (const u32x4*)(K + ((size_t)g * SK + (size_t)s * 1024) * DH) + wave * 64 + lane;
  const u32x4* vp = (const u32x4*)(V + ((size_t)g * SK + (size_t)s * 1024) * DH) + wave * 64 + lane;
  u32x4 q0 = *(const u32x4*)(q + (2 * g) * DH + 8 * c);
  u32x4 q1 = *(const u32x4*)(q + (2 * g + 1) * DH + 8 * c);
  float m0 = -INFINITY, m1 = -INFINITY;
  for (int i = 0; i < 64; ++i) {
    u32x4 kk = __builtin_nontemporal_load(kp + i * 512);
    float d0 = 0.f, d1 = 0.f;
    for (int k = 0; k < 4; ++k) { d0 = dot2(kk[k], q0[k], d0); d1 = dot2(kk[k], q1[k], d1); }
    for (int o = 16; o > 0; o >>= 1) { d0 += __shfl_xor(d0, o); d1 += __shfl_xor(d1, o); }
    d0 *= 0.0625f; d1 *= 0.0625f;
    if (c == 0) { sp[wave][i * 4 + half * 2 + 0] = d0; sp[wave][i * 4 + half * 2 + 1] = d1; }
    m0 = fmaxf(m0, d0); m1 = fmaxf(m1, d1);
  }
  m0 = fmaxf(m0, __shfl_xor(m0, 32)); m1 = fmaxf(m1, __shfl_xor(m1, 32));
  __syncthreads();
  float l0 = 0.f, l1 = 0.f;
  float acc0[8] = {0}, acc1[8] = {0};
  for (int i = 0; i < 64; ++i) {
    u32x4 vv = __builtin_nontemporal_load(vp + i * 512);
    float p0 = __expf(sp[wave][i * 4 + half * 2 + 0] - m0), p1 = __expf(sp[wave][i * 4 + half * 2 + 1] - m1);
    l0 += p0; l1 += p1;
    for (int k = 0; k < 4; ++k) {
      float a = __builtin_bit_cast(float, vv[k] << 16), b = __builtin_bit_cast(float, vv[k] & 0xffff0000u);
      acc0[2 * k] = fmaf(p0, a, acc0[2 * k]); acc0[2 * k + 1] = fmaf(p0, b, acc0[2 * k + 1]);
      acc1[2 * k] = fmaf(p1, a, acc1[2 * k]); acc1[2 * k + 1] = fmaf(p1, b, acc1[2 * k + 1]);
    }
  }
  l0 += __shfl_xor(l0, 32); l1 += __shfl_xor(l1, 32);
  for (int k = 0; k < 8; ++k) { acc0[k] += __shfl_xor(acc0[k], 32); acc1[k] += __shfl_xor(acc1[k], 32); }
  if (half == 0) for (int k = 0; k < 8; ++k) { sacc[wave][8 * c + k] = acc0[k]; sacc[wave][256 + 8 * c + k] = acc1[k]; }
  if (lane == 0) { sml[wave][0] = m0; sml[wave][1] = l0; sml[wave][2] = m1; sml[wave][3] = l1; }
  __syncthreads();
  int t = threadIdx.x, hh = t >> 8;
  float M = -INFINITY;
  for (int w = 0; w < 8; ++w) M = fmaxf(M, sml[w][2 * hh]);
  float L = 0.f, a = 0.f;
  for (int w = 0; w < 8; ++w) { float f = __expf(sml[w][2 * hh] - M); L = fmaf(f, sml[w][2 * hh + 1], L); a = fmaf(f, sacc[w][t], a); }
  int head = 2 * g + hh;
  pacc[((size_t)head * NSPLIT + s) * 256 + (t & 255)] = a;
  if ((t & 255) == 0) { pml[(head * NSPLIT + s) * 2] = M; pml[(head * NSPLIT + s) * 2 + 1] = L; }
}

__global__ void __launch_bounds__(256) k3(const float* pacc, const float* pml, float* o) {
  int head = blockIdx.x, t = threadIdx.x;
  float M = -INFINITY;
  for (int s = 0; s < NSPLIT; ++s) M = fmaxf(M, pml[(head * NSPLIT + s) * 2]);
  float L = 0.f, a = 0.f;
  for (int s = 0; s < NSPLIT; ++s) {
    float f = __expf(pml[(head * NSPLIT + s) * 2] - M);
    L = fmaf(f, pml[(head * NSPLIT + s) * 2 + 1], L);
    a = fmaf(f, pacc[((size_t)head * NSPLIT + s) * 256 + t], a);
  }
  float r = a / L;
  o[head * 256 + t] = r;
}

__global__ void __launch_bounds__(512) k4(const unsigned short* x, const float* o, const float* h, const unsigned short* wo,
                                          const unsigned short* wd, unsigned short* out) {
  __shared__ float ra[8][64], rf[8][64];
  int lane = threadIdx.x & 63, wave = threadIdx.x >> 6, c = blockIdx.x * 64 + lane;
  float a = 0.f, f = 0.f;
  for (int r = wave; r < 4096; r += 8) a = fmaf(o[r], bf2f(wo[(size_t)r * D + c]), a);
  for (int r = wave; r < FF; r += 8) f = fmaf(h[r], bf2f(wd[(size_t)r * D + c]), f);
  ra[wave][lane] = a; rf[wave][lane] = f;
  __syncthreads();
  if (wave == 0) {
    a = 0.f; f = 0.f;
    for (int w = 0; w < 8; ++w) { a += ra[w][lane]; f += rf[w][lane]; }
    float r = bf2f(x[c]) + a + f;
    out[c] = f2bf(r);
  }
}

extern "C" void kernel_launch(void* const* d_in, const int* in_sizes, int n_in, void* d_out, int out_size, void* d_ws,
                              size_t ws_size, hipStream_t stream) {
  const unsigned short* x = (const unsigned short*)d_in[0];
  const unsigned short* K = (const unsigned short*)d_in[1];
  const unsigned short* V = (const unsigned short*)d_in[2];
  const unsigned short* wq = (const unsigned short*)d_in[3];
  const unsigned short* wo = (const unsigned short*)d_in[4];
  const unsigned short* wg = (const unsigned short*)d_in[6];
  const unsigned short* wu = (const unsigned short*)d_in[7];
  const unsigned short* wd = (const unsigned short*)d_in[8];
  char* ws = (char*)d_ws;
  unsigned short* q = (unsigned short*)ws;
  float* h = (float*)(ws + 16384);
  float* o = (float*)(ws + 32768);
  float* pml = (float*)(ws + 65536);
  float* pacc = (float*)(ws + 131072);
  k1<<<23, 256, 0, stream>>>(x, wq, wg, wu, q, h);
  k2<<<256, 512, 0, stream>>>(K, V, q, pacc, pml);
  k3<<<16, 256, 0, stream>>>(pacc, pml, o);
  k4<<<7, 512, 0, stream>>>(x, o, h, wo, wd, (unsigned short*)d_out);
}
```

```cpp
#include <hip/hip_runtime.h>
#include <stdint.h>

typedef unsigned u32x4 __attribute__((ext_vector_type(4)));
typedef unsigned u32x2 __attribute__((ext_vector_type(2)));
typedef float f32x2 __attribute__((ext_vector_type(2)));
typedef float f32x4 __attribute__((ext_vector_type(4)));
typedef __bf16 bf16x2 __attribute__((ext_vector_type(2)));
#define LAS __attribute__((address_space(3)))
typedef __amdgpu_buffer_rsrc_t rsrc_t;

#define S_STATIC 24
#define AHEAD 2
#define NBACK 6
#define ROT 0x100000u
#define WQ_AUX 2
#define BAR_FIRST 1
#define GU_NT 1
#define LATE_W 0

constexpr unsigned REG = 4u << 20;
constexpr int NCH = 256;
constexpr unsigned OOB = 0x40000000u;
constexpr int SPIN_MAX = 200000;
constexpr unsigned WS_EPOCH = 0, WS_CTR = 4096, WS_Q = 16384, WS_ML = 32768, WS_H1 = 65536, WS_H2 = 65536 + (1u << 20);
constexpr unsigned WS_USED = WS_H2 + (1u << 20);
constexpr float CSC = 0.0625f * 1.4426950408889634f;

__device__ __forceinline__ float u2f(unsigned u) { return __builtin_bit_cast(float, u); }
__device__ __forceinline__ unsigned f2u(float f) { return __builtin_bit_cast(unsigned, f); }
__device__ __forceinline__ float rbf(float f) { __bf16 b = (__bf16)f; return (float)b; }
__device__ __forceinline__ unsigned pkbf(float lo, float hi) {
  bf16x2 v; v[0] = (__bf16)lo; v[1] = (__bf16)hi; return __builtin_bit_cast(unsigned, v);
}
__device__ __forceinline__ float dot2(unsigned a, unsigned b, float acc) {
  return __builtin_amdgcn_fdot2_f32_bf16(__builtin_bit_cast(bf16x2, a), __builtin_bit_cast(bf16x2, b), acc, false);
}
template <int CTRL> __device__ __forceinline__ float dpp(float x) {
  return __builtin_bit_cast(float, __builtin_amdgcn_mov_dpp(__builtin_bit_cast(int, x), CTRL, 0xf, 0xf, true));
}
constexpr int XOR1 = 0xB1, XOR2 = 0x4E, XOR7 = 0x141, ROR8 = 0x128, ROR4 = 0x124, BC0 = 0x150, BC8 = 0x158;
__device__ __forceinline__ float sw16_add(float a, float b) {
  auto s = __builtin_amdgcn_permlane16_swap(f2u(a), f2u(b), false, false);
  unsigned s0 = s[0], s1 = s[1]; return u2f(s0) + u2f(s1);
}
__device__ __forceinline__ float sw32_add(float a, float b) {
  auto s = __builtin_amdgcn_permlane32_swap(f2u(a), f2u(b), false, false);
  unsigned s0 = s[0], s1 = s[1]; return u2f(s0) + u2f(s1);
}
__device__ __forceinline__ float sw16_max(float a) {
  auto s = __builtin_amdgcn_permlane16_swap(f2u(a), f2u(a), false, false);
  unsigned s0 = s[0], s1 = s[1]; return __builtin_fmaxf(u2f(s0), u2f(s1));
}
__device__ __forceinline__ float sw32_max(float a) {
  auto s = __builtin_amdgcn_permlane32_swap(f2u(a), f2u(a), false, false);
  unsigned s0 = s[0], s1 = s[1]; return __builtin_fmaxf(u2f(s0), u2f(s1));
}
__device__ __forceinline__ float wave_sum(float t) {
  t += dpp<XOR1>(t); t += dpp<XOR2>(t); t += dpp<XOR7>(t); t += dpp<ROR8>(t);
  t = sw16_add(t, t); return sw32_add(t, t);
}
__device__ __forceinline__ float rdl(float v, int l) { return u2f(__builtin_amdgcn_readlane(f2u(v), l)); }
#define LDL(p) __hip_atomic_load((LAS int*)(p), __ATOMIC_RELAXED, __HIP_MEMORY_SCOPE_WORKGROUP)
#define STL(p, v) __hip_atomic_store((LAS int*)(p), v, __ATOMIC_RELAXED, __HIP_MEMORY_SCOPE_WORKGROUP)
#define SCHED() __builtin_amdgcn_sched_barrier(0)

#define TS(i) do {} while (0)

template <bool REFILL>
__device__ __forceinline__ void step(u32x4 (&kr)[2], u32x4 (&vr)[2], const u32x4& q0, const u32x4& q1, f32x2 (&a0)[4],
                                     f32x2 (&a1)[4], float& m, float& mc, float& l, rsrc_t rk, rsrc_t rv, int voff,
                                     unsigned o, bool lane8) {
  float d[4];
#pragma unroll
  for (int u = 0; u < 2; ++u) {
    const u32x4 k = kr[u];
    float x = 0.f, y = 0.f;
#pragma unroll
    for (int e = 0; e < 4; ++e) { unsigned w = k[e], qa = q0[e], qb = q1[e]; x = dot2(w, qa, x); y = dot2(w, qb, y); }
    if (REFILL) {
      asm volatile("" : "+v"(x), "+v"(y) :: "memory");
      kr[u] = __builtin_amdgcn_raw_buffer_load_b128(rk, voff + u * 1024, o, 2);
      SCHED();
    }
    d[2 * u] = x; d[2 * u + 1] = y;
  }
  float ab = sw16_add(d[0], d[1]), cd = sw16_add(d[2], d[3]);
  ab += dpp<ROR8>(ab); cd += dpp<ROR8>(cd);
  float t = lane8 ? cd : ab;
  t += dpp<XOR7>(t); t += dpp<XOR2>(t); t += dpp<XOR1>(t);
  t = rbf(t);
  if (__builtin_amdgcn_ballot_w64(t > m) != 0) {
    float mx = __builtin_fmaxf(t, dpp<ROR8>(t));
    mx = __builtin_fmaxf(sw32_max(mx), m);
    const float mcn = mx * CSC;
    const float f = __builtin_amdgcn_exp2f(mc - mcn);
    m = mx; mc = mcn; l *= f;
    const float f0 = rdl(f, 0), f1 = rdl(f, 16);
#pragma unroll
    for (int k = 0; k < 4; ++k) { a0[k] *= f0; a1[k] *= f1; }
  }
  const float p = __builtin_amdgcn_exp2f(__builtin_fmaf(t, CSC, -mc));
  l += p;
  auto s = __builtin_amdgcn_permlane16_swap(f2u(p), f2u(p), false, false);
  unsigned s0 = s[0], s1 = s[1];
  const float ph0 = u2f(s0), ph1 = u2f(s1);
  const float pp[4] = {dpp<BC0>(ph0), dpp<BC0>(ph1), dpp<BC8>(ph0), dpp<BC8>(ph1)};
#pragma unroll
  for (int u = 0; u < 2; ++u) {
    const u32x4 v = vr[u];
    const f32x2 p0 = {pp[2 * u], pp[2 * u]}, p1 = {pp[2 * u + 1], pp[2 * u + 1]};
#pragma unroll
    for (int e = 0; e < 4; ++e) {
      unsigned w = v[e];
      const f32x2 x = {u2f(w << 16), u2f(w & 0xffff0000u)};
      a0[e] = __builtin_elementwise_fma(x, p0, a0[e]);
      a1[e] = __builtin_elementwise_fma(x, p1, a1[e]);
    }
    if (REFILL) {
      asm volatile("" : "+v"(a0[0]), "+v"(a0[1]), "+v"(a0[2]), "+v"(a0[3]), "+v"(a1[0]), "+v"(a1[1]), "+v"(a1[2]), "+v"(a1[3]) :: "memory");
      vr[u] = __builtin_amdgcn_raw_buffer_load_b128(rv, voff + u * 1024, o, 2);
      SCHED();
    }
  }
}

__device__ __forceinline__ void gemv16(const u32x4 (&w)[14], const unsigned (&xp)[7], float& r0, float& r1) {
  float a[8];
#pragma unroll
  for (int i = 0; i < 8; ++i) a[i] = 0.f;
#pragma unroll
  for (int j = 0; j < 7; ++j) {
#pragma unroll
    for (int k = 0; k < 4; ++k) {
      unsigned w0 = w[2 * j][k], w1 = w[2 * j + 1][k];
      a[2 * k] = dot2(__builtin_amdgcn_perm(w1, w0, 0x05040100u), xp[j], a[2 * k]);
      a[2 * k + 1] = dot2(__builtin_amdgcn_perm(w1, w0, 0x07060302u), xp[j], a[2 * k + 1]);
    }
  }
  float b[4];
#pragma unroll
  for (int i = 0; i < 4; ++i) b[i] = sw32_add(a[i], a[i + 4]);
  r0 = sw16_add(b[0], b[2]); r1 = sw16_add(b[1], b[3]);
  r0 += dpp<ROR8>(r0); r1 += dpp<ROR8>(r1);
  r0 += dpp<ROR4>(r0); r1 += dpp<ROR4>(r1);
  r0 += dpp<XOR2>(r0); r1 += dpp<XOR2>(r1);
}

__global__ void __launch_bounds__(576) fused_decode(const unsigned* __restrict__ x32, const char* __restrict__ K,
                                                    const char* __restrict__ V, const char* __restrict__ wq,
                                                    const char* __restrict__ wo, const char* __restrict__ wg,
                                                    const char* __restrict__ wu, const char* __restrict__ wd,
                                                    unsigned* __restrict__ out32, char* ws) {
  __shared__ __attribute__((aligned(16))) float macc[8 * 512];
  __shared__ __attribute__((aligned(16))) unsigned qs[256];
  __shared__ __attribute__((aligned(16))) float mml[32];
  __shared__ int ids[264];
  __shared__ int ctl[4];
  const int lane = threadIdx.x & 63, hwave = __builtin_amdgcn_readfirstlane(threadIdx.x >> 6);
  const int wave = hwave == 0 ? 8 : hwave - 1;
  const int b = blockIdx.x, grp = b >> 3, mem = b & 7;
  const int ub = mem * 28 + grp;
  asm volatile("" :: "s"(x32), "s"(K), "s"(V), "s"(wq), "s"(wo), "s"(wg), "s"(wu), "s"(wd), "s"(out32), "s"(ws));
  TS(0);
  const rsrc_t rw = __builtin_amdgcn_make_buffer_rsrc((void*)ws, 0, WS_USED, 0x00020000);
  unsigned tag;

  if (__builtin_expect(wave < 8, 1)) {
    const rsrc_t rk = __builtin_amdgcn_make_buffer_rsrc((void*)(K + (size_t)grp * REG), 0, REG, 0x00020000);
    const rsrc_t rv = __builtin_amdgcn_make_buffer_rsrc((void*)(V + (size_t)grp * REG), 0, REG, 0x00020000);
    const int voff = lane * 16 + wave * 2048;
    const unsigned rot = grp * ROT;
    auto offs = [&](int id) -> unsigned { return id < NCH ? ((id * 16384u + rot) & (REG - 1)) : OOB; };
    u32x4 kb[NBACK + 1][2], vb[NBACK + 1][2];
    if (BAR_FIRST) { asm volatile("s_barrier" ::: "memory"); SCHED(); }
#pragma unroll
    for (int s = 0; s <= NBACK; ++s) {
      const unsigned o = offs(s * 8 + mem);
#pragma unroll
      for (int u = 0; u < 2; ++u) {
        if (s > 0) asm volatile("s_waitcnt vmcnt(3)" ::: "memory");
        kb[s][u] = __builtin_amdgcn_raw_buffer_load_b128(rk, voff + u * 1024, o, 2);
        SCHED();
      }
#pragma unroll
      for (int u = 0; u < 2; ++u) {
        if (s > 0) asm volatile("s_waitcnt vmcnt(3)" ::: "memory");
        vb[s][u] = __builtin_amdgcn_raw_buffer_load_b128(rv, voff + u * 1024, o, 2);
        SCHED();
      }
      if (!BAR_FIRST && s == 0) { asm volatile("s_barrier" ::: "memory"); SCHED(); }
    }
    TS(1);
    {
      int spin = 0;
#pragma unroll 1
      while (__builtin_amdgcn_readfirstlane(LDL(&ctl[1])) == 0 && ++spin < SPIN_MAX) __builtin_amdgcn_s_sleep(1);
    }
    asm volatile("" ::: "memory");
    TS(2);
    const u32x4 q0 = *(const u32x4*)&qs[(lane & 31) * 4], q1 = *(const u32x4*)&qs[128 + (lane & 31) * 4];
    tag = __builtin_amdgcn_readfirstlane(LDL(&ctl[2]));
    f32x2 a0[4], a1[4];
#pragma unroll
    for (int k = 0; k < 4; ++k) { a0[k] = f32x2{0.f, 0.f}; a1[k] = f32x2{0.f, 0.f}; }
    float m = -INFINITY, mc = -INFINITY, l = 0.f;
    const bool lane8 = (lane & 8) != 0;
#pragma unroll
    for (int s = 0; s < NBACK; ++s) {
      step<true>(kb[NBACK], vb[NBACK], q0, q1, a0, a1, m, mc, l, rk, rv, voff, offs((NBACK + 1 + s) * 8 + mem), lane8);
      step<false>(kb[s], vb[s], q0, q1, a0, a1, m, mc, l, rk, rv, voff, 0, lane8);
      SCHED();
    }
    TS(3);
    int n = 2 * NBACK, nxt = (2 * NBACK + 1) * 8 + mem;
#pragma unroll 1
    for (;;) {
      if (lane == 0) __hip_atomic_fetch_max(&ctl[0], n, __ATOMIC_RELAXED, __HIP_MEMORY_SCOPE_WORKGROUP);
      step<true>(kb[NBACK], vb[NBACK], q0, q1, a0, a1, m, mc, l, rk, rv, voff, offs(nxt), lane8);
      if (nxt >= NCH) break;
      int id = __builtin_amdgcn_readfirstlane(LDL(&ids[n + 2]));
      int spin = 0;
#pragma unroll 1
      while (id < 0 && ++spin < SPIN_MAX) { __builtin_amdgcn_s_sleep(1); id = __builtin_amdgcn_readfirstlane(LDL(&ids[n + 2])); }
      nxt = id < 0 ? NCH : id;
      ++n;
    }
    TS(4);
    f32x4 lo, hi;
    lo[0] = sw32_add(a0[0][0], a1[0][0]); lo[1] = sw32_add(a0[0][1], a1[0][1]);
    lo[2] = sw32_add(a0[1][0], a1[1][0]); lo[3] = sw32_add(a0[1][1], a1[1][1]);
    hi[0] = sw32_add(a0[2][0], a1[2][0]); hi[1] = sw32_add(a0[2][1], a1[2][1]);
    hi[2] = sw32_add(a0[3][0], a1[3][0]); hi[3] = sw32_add(a0[3][1], a1[3][1]);
    *(f32x4*)&macc[wave * 512 + lane * 8] = lo;
    *(f32x4*)&macc[wave * 512 + lane * 8 + 4] = hi;
    l += dpp<ROR8>(l); l = sw32_add(l, l);
    if ((lane & 47) == 0) *(f32x2*)&mml[(lane >> 4) * 16 + wave * 2] = f32x2{mc, l};
    asm volatile("s_waitcnt lgkmcnt(0)\n s_barrier" ::: "memory");
    TS(5);
    if (wave < 4) {
      const int t = wave * 64 + lane, h = wave >> 1;
      f32x4 ml[4]; f32x2 av[8];
#pragma unroll
      for (int i = 0; i < 4; ++i) ml[i] = *(const f32x4*)&mml[h * 16 + i * 4];
#pragma unroll
      for (int w = 0; w < 8; ++w) av[w] = *(const f32x2*)&macc[w * 512 + 2 * t];
      float M = ml[0][0];
#pragma unroll
      for (int i = 0; i < 4; ++i) M = __builtin_fmaxf(M, __builtin_fmaxf(ml[i][0], ml[i][2]));
      float L = 0.f; f32x2 acc = {0.f, 0.f};
#pragma unroll
      for (int w = 0; w < 8; ++w) {
        const float f = __builtin_amdgcn_exp2f(ml[w >> 1][(w & 1) * 2] - M);
        L = __builtin_fmaf(f, ml[w >> 1][(w & 1) * 2 + 1], L);
        acc = __builtin_elementwise_fma(av[w], f32x2{f, f}, acc);
      }
      const float ax = acc[0], ay = acc[1];
      __builtin_amdgcn_raw_buffer_store_b128(u32x4{f2u(ax), tag, f2u(ay), tag}, rw, t * 16, WS_H1 + b * 4096, 16);
      if ((t & 127) == 0) __builtin_amdgcn_raw_buffer_store_b128(u32x4{f2u(M), tag, f2u(L), tag}, rw, h * 16, WS_ML + b * 64, 16);
      TS(6);
    } else if (wave == 4 && grp < 28) {
      const int ci = ub / 56, cl = ub % 56;
      const unsigned xv = x32[cl * 4 + ci];
      u32x4 g[4];
#pragma unroll 1
      for (int spin = 0; spin < SPIN_MAX; ++spin) {
        bool ok = true;
#pragma unroll
        for (int k = 0; k < 4; ++k) g[k] = __builtin_amdgcn_raw_buffer_load_b128(rw, lane * 4096, WS_H2 + k * 262144 + ci * 1024 + cl * 16, 16);
#pragma unroll
        for (int k = 0; k < 4; ++k) ok &= (g[k][1] == tag) & (g[k][3] == tag);
        if (__builtin_amdgcn_ballot_w64(!ok) == 0) break;
        __builtin_amdgcn_s_sleep(2);
      }
      TS(6);
      float s0 = 0.f, s1 = 0.f;
#pragma unroll
      for (int k = 0; k < 4; ++k) { unsigned g0 = g[k][0], g2 = g[k][2]; s0 += u2f(g0); s1 += u2f(g2); }
      float s = sw32_add(s0, s1);
      s += dpp<XOR1>(s); s += dpp<XOR2>(s); s += dpp<XOR7>(s); s += dpp<ROR8>(s); s = sw16_add(s, s);
      const float o0 = rdl(s, 0) + u2f(xv << 16), o1 = rdl(s, 32) + u2f(xv & 0xffff0000u);
      if (lane == 0) {
        out32[cl * 4 + ci] = pkbf(o0, o1);
        if (ub == 0) __builtin_amdgcn_raw_buffer_store_b32(tag, rw, 0, WS_EPOCH, 16);
      }
      TS(7);
    }
    asm volatile("" :: "v"(kb[NBACK][0]), "v"(kb[NBACK][1]), "v"(vb[NBACK][0]), "v"(vb[NBACK][1]));
  } else {
    __builtin_amdgcn_s_setprio(3);
    const int g = b >> 5;
    const unsigned ep = __builtin_amdgcn_raw_buffer_load_b32(rw, 0, WS_EPOCH, 16);
    unsigned xr[7];
#pragma unroll
    for (int j = 0; j < 7; ++j) xr[j] = x32[(lane >> 1) + 32 * j];
    u32x4 w[14];
    const int jq = mem * 4 + (grp & 3);
    {
      const rsrc_t r = __builtin_amdgcn_make_buffer_rsrc((void*)wq, 0, 448 * 8192, 0x00020000);
      const int vo = (lane >> 1) * 16384 + (lane & 1) * 16;
#pragma unroll
      for (int i = 0; i < 14; ++i) w[i] = __builtin_amdgcn_raw_buffer_load_b128(r, vo, g * 1024 + jq * 32 + (i >> 1) * 524288 + (i & 1) * 8192, WQ_AUX);
    }
    if (mem == 0 && lane == 0) __builtin_amdgcn_raw_buffer_store_b32(0u, rw, 0, WS_CTR + grp * 256, 16);
    SCHED();
#pragma unroll
    for (int i = 0; i < 4; ++i) { const int k = lane + 64 * i; ids[k] = k < S_STATIC ? k * 8 + mem : -1; }
    if (lane < 8) ids[256 + lane] = -1;
    if (lane < 4) ctl[lane] = 0;
    asm volatile("s_waitcnt lgkmcnt(0)\n s_barrier" ::: "memory");
    asm volatile("" : "+v"(xr[0]), "+v"(xr[1]), "+v"(xr[2]), "+v"(xr[3]), "+v"(xr[4]), "+v"(xr[5]), "+v"(xr[6]));
    unsigned xp[7];
    {
      float s = 0.f;
#pragma unroll
      for (int j = 0; j < 7; ++j) s += u2f(xr[j] << 16) + u2f(xr[j] & 0xffff0000u);
      const float mean = wave_sum(s) * (1.f / 896.f);
      float v = 0.f;
#pragma unroll
      for (int j = 0; j < 7; ++j) { const float da = u2f(xr[j] << 16) - mean, db = u2f(xr[j] & 0xffff0000u) - mean; v += da * da + db * db; }
      const float var = wave_sum(v) * (1.f / 896.f);
      const float mb = rbf(mean), rden = __builtin_amdgcn_rcpf(rbf(__builtin_amdgcn_sqrtf(rbf(rbf(var) + 1e-5f))));
#pragma unroll
      for (int j = 0; j < 7; ++j) xp[j] = pkbf(rbf(u2f(xr[j] << 16) - mb) * rden, rbf(u2f(xr[j] & 0xffff0000u) - mb) * rden);
    }
    tag = __builtin_amdgcn_readfirstlane(ep) + 1u;
    TS(1);
    {
      float r0, r1;
      gemv16(w, xp, r0, r1);
      const unsigned pk = pkbf(r0, r1);
      auto s = __builtin_amdgcn_permlane16_swap(pk, pk, false, false);
      unsigned s0 = s[0], s1 = s[1];
      asm volatile("s_waitcnt vmcnt(0)" : "+v"(s0), "+v"(s1) :: "memory");
      if ((lane & 30) == 0)
        __builtin_amdgcn_raw_buffer_store_b128(u32x4{s0, tag, s1, tag}, rw, ((lane & 1) * 2 + (lane >> 5)) * 16, WS_Q + g * 2048 + jq * 64, 16);
    }
    TS(2);
    {
      u32x4 g0, g1;
#pragma unroll 1
      for (int spin = 0; spin < SPIN_MAX; ++spin) {
        g0 = __builtin_amdgcn_raw_buffer_load_b128(rw, lane * 16 + g * 2048 + WS_Q, 0, 16);
        g1 = __builtin_amdgcn_raw_buffer_load_b128(rw, lane * 16 + g * 2048 + WS_Q + 1024, 0, 16);
        const bool ok = (g0[1] == tag) & (g0[3] == tag) & (g1[1] == tag) & (g1[3] == tag);
        if (__builtin_amdgcn_ballot_w64(!ok) == 0) break;
        __builtin_amdgcn_s_sleep(1);
      }
      *(u32x2*)&qs[lane * 2] = u32x2{g0[0], g0[2]};
      *(u32x2*)&qs[128 + lane * 2] = u32x2{g1[0], g1[2]};
      if (lane == 0) STL(&ctl[2], (int)tag);
      asm volatile("s_waitcnt lgkmcnt(0)" ::: "memory");
      if (lane == 0) STL(&ctl[1], 1);
    }
    TS(3);
    float fo[8];
    unsigned wp[8][8];
    auto tail_weights = [&]() {
#pragma unroll
      for (int i = 0; i < 8; ++i) fo[i] = 0.f;
      if (grp < 28) {
        const char* base = ((lane & 1) ? wu : wg) + (lane >> 1) * 7168 + ub * 16;
#pragma unroll
        for (int i = 0; i < 14; ++i) {
          const u32x4* wptr = (const u32x4*)(base + (i >> 1) * 229376 + (i & 1) * 3584);
          w[i] = GU_NT ? __builtin_nontemporal_load(wptr) : *wptr;
        }
        const rsrc_t r = __builtin_amdgcn_make_buffer_rsrc((void*)wd, 0, 1792 * 896, 0x00020000);
        u32x4 dv[8];
#pragma unroll
        for (int i = 0; i < 8; ++i) dv[i] = __builtin_amdgcn_raw_buffer_load_b128(r, lane * 16, (ub * 8 + i) * 896, 2);
        float r0, r1;
        gemv16(w, xp, r0, r1);
        r0 = rbf(r0); r1 = rbf(r1);
        const float u0 = dpp<XOR1>(r0), u1 = dpp<XOR1>(r1);
        const float s0 = rbf(r0 * __builtin_amdgcn_rcpf(1.f + __expf(-r0))), s1 = rbf(r1 * __builtin_amdgcn_rcpf(1.f + __expf(-r1)));
        const unsigned hp = pkbf(s0 * u0, s1 * u1);
#pragma unroll
        for (int pc = 0; pc < 4; ++pc) {
          const unsigned hs = __builtin_amdgcn_readlane(hp, pc * 16);
#pragma unroll
          for (int k = 0; k < 4; ++k) {
            unsigned w0 = dv[2 * pc][k], w1 = dv[2 * pc + 1][k];
            fo[2 * k] = dot2(__builtin_amdgcn_perm(w1, w0, 0x05040100u), hs, fo[2 * k]);
            fo[2 * k + 1] = dot2(__builtin_amdgcn_perm(w1, w0, 0x07060302u), hs, fo[2 * k + 1]);
          }
        }
      }
      SCHED();
      const rsrc_t r = __builtin_amdgcn_make_buffer_rsrc((void*)wo, 0, 4096 * 896, 0x00020000);
      u32x4 ov[16];
#pragma unroll
      for (int i = 0; i < 16; ++i) ov[i] = __builtin_amdgcn_raw_buffer_load_b128(r, lane * 16, (b * 16 + i) * 896, 2);
#pragma unroll
      for (int pc = 0; pc < 8; ++pc)
#pragma unroll
        for (int k = 0; k < 4; ++k) {
          unsigned w0 = ov[2 * pc][k], w1 = ov[2 * pc + 1][k];
          wp[pc][2 * k] = __builtin_amdgcn_perm(w1, w0, 0x05040100u);
          wp[pc][2 * k + 1] = __builtin_amdgcn_perm(w1, w0, 0x07060302u);
          asm volatile("" : "+v"(wp[pc][2 * k]), "+v"(wp[pc][2 * k + 1]));
        }
      SCHED();
    };
    if (!LATE_W) tail_weights();
    TS(4);
    {
      const rsrc_t rc = __builtin_amdgcn_make_buffer_rsrc((void*)(ws + WS_CTR + grp * 256), 0, 4, 0x00020000);
      int k = S_STATIC;
#pragma unroll 1
      for (int spin = 0; spin < 16 * SPIN_MAX && k < 260; ++spin) {
        const int ld = __builtin_amdgcn_readfirstlane(LDL(&ctl[0]));
        if (k <= ld + AHEAD) {
          const int p = __builtin_amdgcn_raw_ptr_buffer_atomic_add_i32(1, rc, lane * 4, 0, 16);
          const unsigned id = (unsigned)__builtin_amdgcn_readfirstlane(p) + S_STATIC * 8;
          const int idc = id < (unsigned)NCH ? (int)id : NCH;
          if (lane == 0) STL(&ids[k], idc);
          ++k;
          if (idc >= NCH) break;
        } else {
          __builtin_amdgcn_s_sleep(2);
        }
      }
    }
    TS(5);
    if (LATE_W) tail_weights();
    asm volatile("s_waitcnt lgkmcnt(0)\n s_barrier" ::: "memory");
    TS(6);
    {
      const int j1 = b & 31;
      u32x4 ga[4], gm[4];
      const int vo = (lane >> 3) * 4096 + (lane & 7) * 16, vm = (lane >> 3) * 64;
#pragma unroll 1
      for (int spin = 0; spin < SPIN_MAX; ++spin) {
#pragma unroll
        for (int i = 0; i < 4; ++i) {
          ga[i] = __builtin_amdgcn_raw_buffer_load_b128(rw, vo, WS_H1 + (g * 32 + i * 8) * 4096 + j1 * 128, 16);
          gm[i] = __builtin_amdgcn_raw_buffer_load_b128(rw, vm, WS_ML + (g * 32 + i * 8) * 64 + (j1 >> 4) * 16, 16);
        }
        bool ok = true;
#pragma unroll
        for (int i = 0; i < 4; ++i) ok &= (ga[i][1] == tag) & (ga[i][3] == tag) & (gm[i][1] == tag) & (gm[i][3] == tag);
        if (__builtin_amdgcn_ballot_w64(!ok) == 0) break;
        __builtin_amdgcn_s_sleep(1);
      }
      TS(7);
      float mv[4];
#pragma unroll
      for (int i = 0; i < 4; ++i) { unsigned t0 = gm[i][0]; mv[i] = u2f(t0); }
      float M = __builtin_fmaxf(__builtin_fmaxf(mv[0], mv[1]), __builtin_fmaxf(mv[2], mv[3]));
      M = __builtin_fmaxf(M, dpp<ROR8>(M)); M = sw16_max(M); M = sw32_max(M);
      float L = 0.f, s0 = 0.f, s1 = 0.f;
#pragma unroll
      for (int i = 0; i < 4; ++i) {
        const float f = __builtin_amdgcn_exp2f(mv[i] - M);
        unsigned t0 = ga[i][0], t1 = ga[i][2], t2 = gm[i][2];
        s0 = __builtin_fmaf(f, u2f(t0), s0); s1 = __builtin_fmaf(f, u2f(t1), s1); L = __builtin_fmaf(f, u2f(t2), L);
      }
      s0 += dpp<ROR8>(s0); s1 += dpp<ROR8>(s1); L += dpp<ROR8>(L);
      s0 = sw16_add(s0, s0); s1 = sw16_add(s1, s1); L = sw16_add(L, L);
      s0 = sw32_add(s0, s0); s1 = sw32_add(s1, s1); L = sw32_add(L, L);
      const float rl = __builtin_amdgcn_rcpf(L);
      const unsigned op = pkbf(s0 * rl, s1 * rl);
#pragma unroll
      for (int pc = 0; pc < 8; ++pc) {
        const unsigned os = __builtin_amdgcn_readlane(op, pc);
#pragma unroll
        for (int c = 0; c < 8; ++c) fo[c] = dot2(wp[pc][c], os, fo[c]);
      }
      if (lane < 56) {
#pragma unroll
        for (int i = 0; i < 4; ++i)
          __builtin_amdgcn_raw_buffer_store_b128(u32x4{f2u(fo[2 * i]), tag, f2u(fo[2 * i + 1]), tag}, rw, lane * 16, WS_H2 + b * 4096 + i * 1024, 16);
      }
      TS(8);
    }
  }
}

extern "C" void kernel_launch(void* const* d_in, const int* in_sizes, int n_in, void* d_out, int out_size, void* d_ws,
                              size_t ws_size, hipStream_t stream) {
  fused_decode<<<256, 576, 0, stream>>>((const unsigned*)d_in[0], (const char*)d_in[1], (const char*)d_in[2],
                                        (const char*)d_in[3], (const char*)d_in[4], (const char*)d_in[6],
                                        (const char*)d_in[7], (const char*)d_in[8], (unsigned*)d_out, (char*)d_ws);
}
```

```cpp
#define WQ_AUX 0
#define GU_NT 0
#define WO_AUX 0
#include <hip/hip_runtime.h>
#include <stdint.h>

typedef unsigned u32x4 __attribute__((ext_vector_type(4)));
typedef unsigned u32x2 __attribute__((ext_vector_type(2)));
typedef float f32x2 __attribute__((ext_vector_type(2)));
typedef float f32x4 __attribute__((ext_vector_type(4)));
typedef __bf16 bf16x2 __attribute__((ext_vector_type(2)));
#define LAS __attribute__((address_space(3)))
typedef __amdgpu_buffer_rsrc_t rsrc_t;

#define S_STATIC 24
#define AHEAD 2
#define NBACK 6
#define ROT 0x100000u
#define BAR_FIRST 1
#define MAP 0
#define K_AUX 2
#define V_AUX 2
#define LATE_W 0

constexpr unsigned REG = 4u << 20;
constexpr int NCH = 256;
constexpr unsigned OOB = 0x40000000u;
constexpr int SPIN_MAX = 200000;
constexpr unsigned WS_EPOCH = 0, WS_CTR = 4096, WS_Q = 16384, WS_ML = 32768, WS_H1 = 65536, WS_H2 = 65536 + (1u << 20);
constexpr unsigned WS_USED = WS_H2 + (1u << 20);
constexpr float CSC = 0.0625f * 1.4426950408889634f;

__device__ __forceinline__ float u2f(unsigned u) { return __builtin_bit_cast(float, u); }
__device__ __forceinline__ unsigned f2u(float f) { return __builtin_bit_cast(unsigned, f); }
__device__ __forceinline__ float rbf(float f) { __bf16 b = (__bf16)f; return (float)b; }
__device__ __forceinline__ unsigned pkbf(float lo, float hi) {
  bf16x2 v; v[0] = (__bf16)lo; v[1] = (__bf16)hi; return __builtin_bit_cast(unsigned, v);
}
__device__ __forceinline__ float dot2(unsigned a, unsigned b, float acc) {
  return __builtin_amdgcn_fdot2_f32_bf16(__builtin_bit_cast(bf16x2, a), __builtin_bit_cast(bf16x2, b), acc, false);
}
template <int CTRL> __device__ __forceinline__ float dpp(float x) {
  return __builtin_bit_cast(float, __builtin_amdgcn_mov_dpp(__builtin_bit_cast(int, x), CTRL, 0xf, 0xf, true));
}
constexpr int XOR1 = 0xB1, XOR2 = 0x4E, XOR7 = 0x141, ROR8 = 0x128, ROR4 = 0x124, BC0 = 0x150, BC8 = 0x158;
__device__ __forceinline__ float sw16_add(float a, float b) {
  auto s = __builtin_amdgcn_permlane16_swap(f2u(a), f2u(b), false, false);
  unsigned s0 = s[0], s1 = s[1]; return u2f(s0) + u2f(s1);
}
__device__ __forceinline__ float sw32_add(float a, float b) {
  auto s = __builtin_amdgcn_permlane32_swap(f2u(a), f2u(b), false, false);
  unsigned s0 = s[0], s1 = s[1]; return u2f(s0) + u2f(s1);
}
__device__ __forceinline__ float sw16_max(float a) {
  auto s = __builtin_amdgcn_permlane16_swap(f2u(a), f2u(a), false, false);
  unsigned s0 = s[0], s1 = s[1]; return __builtin_fmaxf(u2f(s0), u2f(s1));
}
__device__ __forceinline__ float sw32_max(float a) {
  auto s = __builtin_amdgcn_permlane32_swap(f2u(a), f2u(a), false, false);
  unsigned s0 = s[0], s1 = s[1]; return __builtin_fmaxf(u2f(s0), u2f(s1));
}
__device__ __forceinline__ float wave_sum(float t) {
  t += dpp<XOR1>(t); t += dpp<XOR2>(t); t += dpp<XOR7>(t); t += dpp<ROR8>(t);
  t = sw16_add(t, t); return sw32_add(t, t);
}
__device__ __forceinline__ float rdl(float v, int l) { return u2f(__builtin_amdgcn_readlane(f2u(v), l)); }
#define LDL(p) __hip_atomic_load((LAS int*)(p), __ATOMIC_RELAXED, __HIP_MEMORY_SCOPE_WORKGROUP)
#define STL(p, v) __hip_atomic_store((LAS int*)(p), v, __ATOMIC_RELAXED, __HIP_MEMORY_SCOPE_WORKGROUP)
#define SCHED() __builtin_amdgcn_sched_barrier(0)

#define TS(i) do {} while (0)

template <bool REFILL>
__device__ __forceinline__ void step(u32x4 (&kr)[2], u32x4 (&vr)[2], const u32x4& q0, const u32x4& q1, f32x2 (&a0)[4],
                                     f32x2 (&a1)[4], float& m, float& mc, float& l, rsrc_t rk, rsrc_t rv, int voff,
                                     unsigned o, bool lane8) {
  float d[4];
#pragma unroll
  for (int u = 0; u < 2; ++u) {
    const u32x4 k = kr[u];
    float x = 0.f, y = 0.f;
#pragma unroll
    for (int e = 0; e < 4; ++e) { unsigned w = k[e], qa = q0[e], qb = q1[e]; x = dot2(w, qa, x); y = dot2(w, qb, y); }
    if (REFILL) {
      asm volatile("" : "+v"(x), "+v"(y) :: "memory");
      kr[u] = __builtin_amdgcn_raw_buffer_load_b128(rk, voff + u * 1024, o, K_AUX);
      SCHED();
    }
    d[2 * u] = x; d[2 * u + 1] = y;
  }
  float ab = sw16_add(d[0], d[1]), cd = sw16_add(d[2], d[3]);
  ab += dpp<ROR8>(ab); cd += dpp<ROR8>(cd);
  float t = lane8 ? cd : ab;
  t += dpp<XOR7>(t); t += dpp<XOR2>(t); t += dpp<XOR1>(t);
  t = rbf(t);
  if (__builtin_amdgcn_ballot_w64(t > m) != 0) {
    float mx = __builtin_fmaxf(t, dpp<ROR8>(t));
    mx = __builtin_fmaxf(sw32_max(mx), m);
    const float mcn = mx * CSC;
    const float f = __builtin_amdgcn_exp2f(mc - mcn);
    m = mx; mc = mcn; l *= f;
    const float f0 = rdl(f, 0), f1 = rdl(f, 16);
#pragma unroll
    for (int k = 0; k < 4; ++k) { a0[k] *= f0; a1[k] *= f1; }
  }
  const float p = __builtin_amdgcn_exp2f(__builtin_fmaf(t, CSC, -mc));
  l += p;
  auto s = __builtin_amdgcn_permlane16_swap(f2u(p), f2u(p), false, false);
  unsigned s0 = s[0], s1 = s[1];
  const float ph0 = u2f(s0), ph1 = u2f(s1);
  const float pp[4] = {dpp<BC0>(ph0), dpp<BC0>(ph1), dpp<BC8>(ph0), dpp<BC8>(ph1)};
#pragma unroll
  for (int u = 0; u < 2; ++u) {
    const u32x4 v = vr[u];
    const f32x2 p0 = {pp[2 * u], pp[2 * u]}, p1 = {pp[2 * u + 1], pp[2 * u + 1]};
#pragma unroll
    for (int e = 0; e < 4; ++e) {
      unsigned w = v[e];
      const f32x2 x = {u2f(w << 16), u2f(w & 0xffff0000u)};
      a0[e] = __builtin_elementwise_fma(x, p0, a0[e]);
      a1[e] = __builtin_elementwise_fma(x, p1, a1[e]);
    }
    if (REFILL) {
      asm volatile("" : "+v"(a0[0]), "+v"(a0[1]), "+v"(a0[2]), "+v"(a0[3]), "+v"(a1[0]), "+v"(a1[1]), "+v"(a1[2]), "+v"(a1[3]) :: "memory");
      vr[u] = __builtin_amdgcn_raw_buffer_load_b128(rv, voff + u * 1024, o, V_AUX);
      SCHED();
    }
  }
}

__device__ __forceinline__ void gemv16(const u32x4 (&w)[14], const unsigned (&xp)[7], float& r0, float& r1) {
  float a[8];
#pragma unroll
  for (int i = 0; i < 8; ++i) a[i] = 0.f;
#pragma unroll
  for (int j = 0; j < 7; ++j) {
#pragma unroll
    for (int k = 0; k < 4; ++k) {
      unsigned w0 = w[2 * j][k], w1 = w[2 * j + 1][k];
      a[2 * k] = dot2(__builtin_amdgcn_perm(w1, w0, 0x05040100u), xp[j], a[2 * k]);
      a[2 * k + 1] = dot2(__builtin_amdgcn_perm(w1, w0, 0x07060302u), xp[j], a[2 * k + 1]);
    }
  }
  float b[4];
#pragma unroll
  for (int i = 0; i < 4; ++i) b[i] = sw32_add(a[i], a[i + 4]);
  r0 = sw16_add(b[0], b[2]); r1 = sw16_add(b[1], b[3]);
  r0 += dpp<ROR8>(r0); r1 += dpp<ROR8>(r1);
  r0 += dpp<ROR4>(r0); r1 += dpp<ROR4>(r1);
  r0 += dpp<XOR2>(r0); r1 += dpp<XOR2>(r1);
}

__global__ void __launch_bounds__(576) fused_decode(const unsigned* __restrict__ x32, const char* __restrict__ K,
                                                    const char* __restrict__ V, const char* __restrict__ wq,
                                                    const char* __restrict__ wo, const char* __restrict__ wg,
                                                    const char* __restrict__ wu, const char* __restrict__ wd,
                                                    unsigned* __restrict__ out32, char* ws) {
  __shared__ __attribute__((aligned(16))) float macc[8 * 512];
  __shared__ __attribute__((aligned(16))) unsigned qs[256];
  __shared__ __attribute__((aligned(16))) float mml[32];
  __shared__ int ids[264];
  __shared__ int ctl[4];
  const int lane = threadIdx.x & 63, hwave = __builtin_amdgcn_readfirstlane(threadIdx.x >> 6);
  const int wave = hwave == 0 ? 8 : hwave - 1;
  const int b = blockIdx.x, grp = b >> 3, mem = b & 7;
  const int ub = mem * 28 + grp;
  asm volatile("" :: "s"(x32), "s"(K), "s"(V), "s"(wq), "s"(wo), "s"(wg), "s"(wu), "s"(wd), "s"(out32), "s"(ws));
  TS(0);
  const rsrc_t rw = __builtin_amdgcn_make_buffer_rsrc((void*)ws, 0, WS_USED, 0x00020000);
  unsigned tag;

  if (__builtin_expect(wave < 8, 1)) {
    const rsrc_t rk = __builtin_amdgcn_make_buffer_rsrc((void*)(K + (size_t)grp * REG), 0, REG, 0x00020000);
    const rsrc_t rv = __builtin_amdgcn_make_buffer_rsrc((void*)(V + (size_t)grp * REG), 0, REG, 0x00020000);
    const int voff = lane * 16 + wave * ((MAP & 2) ? 4096 : 2048);
    const unsigned rot = grp * ROT;
    auto offs = [&](int id) -> unsigned {
      unsigned c = id;
      if (MAP == 1 || MAP == 3) c = id < S_STATIC * 8 ? (id & 7) * S_STATIC + (id >> 3) : id;
      if (MAP == 2) c = id < S_STATIC * 8 ? (id >> 4) * 16 + (id & 7) * 2 + ((id >> 3) & 1) : id;
      const unsigned o = (MAP & 2) ? (c >> 1) * 32768u + (c & 1) * 2048u : c * 16384u;
      return id < NCH ? ((o + rot) & (REG - 1)) : OOB;
    };
    u32x4 kb[NBACK + 1][2], vb[NBACK + 1][2];
    if (BAR_FIRST) { asm volatile("s_barrier" ::: "memory"); SCHED(); }
#pragma unroll
    for (int s = 0; s <= NBACK; ++s) {
      const unsigned o = offs(s * 8 + mem);
#pragma unroll
      for (int u = 0; u < 2; ++u) {
        if (s > 0) asm volatile("s_waitcnt vmcnt(3)" ::: "memory");
        kb[s][u] = __builtin_amdgcn_raw_buffer_load_b128(rk, voff + u * 1024, o, K_AUX);
        SCHED();
      }
#pragma unroll
      for (int u = 0; u < 2; ++u) {
        if (s > 0) asm volatile("s_waitcnt vmcnt(3)" ::: "memory");
        vb[s][u] = __builtin_amdgcn_raw_buffer_load_b128(rv, voff + u * 1024, o, V_AUX);
        SCHED();
      }
      if (!BAR_FIRST && s == 0) { asm volatile("s_barrier" ::: "memory"); SCHED(); }
    }
    TS(1);
    {
      int spin = 0;
#pragma unroll 1
      while (__builtin_amdgcn_readfirstlane(LDL(&ctl[1])) == 0 && ++spin < SPIN_MAX) __builtin_amdgcn_s_sleep(1);
    }
    asm volatile("" ::: "memory");
    TS(2);
    const u32x4 q0 = *(const u32x4*)&qs[(lane & 31) * 4], q1 = *(const u32x4*)&qs[128 + (lane & 31) * 4];
    tag = __builtin_amdgcn_readfirstlane(LDL(&ctl[2]));
    f32x2 a0[4], a1[4];
#pragma unroll
    for (int k = 0; k < 4; ++k) { a0[k] = f32x2{0.f, 0.f}; a1[k] = f32x2{0.f, 0.f}; }
    float m = -INFINITY, mc = -INFINITY, l = 0.f;
    const bool lane8 = (lane & 8) != 0;
#pragma unroll
    for (int s = 0; s < NBACK; ++s) {
      step<true>(kb[NBACK], vb[NBACK], q0, q1, a0, a1, m, mc, l, rk, rv, voff, offs((NBACK + 1 + s) * 8 + mem), lane8);
      step<false>(kb[s], vb[s], q0, q1, a0, a1, m, mc, l, rk, rv, voff, 0, lane8);
      SCHED();
    }
    TS(3);
    int n = 2 * NBACK, nxt = (2 * NBACK + 1) * 8 + mem;
#pragma unroll 1
    for (;;) {
      if (lane == 0) __hip_atomic_fetch_max(&ctl[0], n, __ATOMIC_RELAXED, __HIP_MEMORY_SCOPE_WORKGROUP);
      step<true>(kb[NBACK], vb[NBACK], q0, q1, a0, a1, m, mc, l, rk, rv, voff, offs(nxt), lane8);
      if (nxt >= NCH) break;
      int id = __builtin_amdgcn_readfirstlane(LDL(&ids[n + 2]));
      int spin = 0;
#pragma unroll 1
      while (id < 0 && ++spin < SPIN_MAX) { __builtin_amdgcn_s_sleep(1); id = __builtin_amdgcn_readfirstlane(LDL(&ids[n + 2])); }
      nxt = id < 0 ? NCH : id;
      ++n;
    }
    TS(4);
    f32x4 lo, hi;
    lo[0] = sw32_add(a0[0][0], a1[0][0]); lo[1] = sw32_add(a0[0][1], a1[0][1]);
    lo[2] = sw32_add(a0[1][0], a1[1][0]); lo[3] = sw32_add(a0[1][1], a1[1][1]);
    hi[0] = sw32_add(a0[2][0], a1[2][0]); hi[1] = sw32_add(a0[2][1], a1[2][1]);
    hi[2] = sw32_add(a0[3][0], a1[3][0]); hi[3] = sw32_add(a0[3][1], a1[3][1]);
    *(f32x4*)&macc[wave * 512 + lane * 8] = lo;
    *(f32x4*)&macc[wave * 512 + lane * 8 + 4] = hi;
    l += dpp<ROR8>(l); l = sw32_add(l, l);
    if ((lane & 47) == 0) *(f32x2*)&mml[(lane >> 4) * 16 + wave * 2] = f32x2{mc, l};
    asm volatile("s_waitcnt lgkmcnt(0)\n s_barrier" ::: "memory");
    TS(5);
    if (wave < 4) {
      const int t = wave * 64 + lane, h = wave >> 1;
      f32x4 ml[4]; f32x2 av[8];
#pragma unroll
      for (int i = 0; i < 4; ++i) ml[i] = *(const f32x4*)&mml[h * 16 + i * 4];
#pragma unroll
      for (int w = 0; w < 8; ++w) av[w] = *(const f32x2*)&macc[w * 512 + 2 * t];
      float M = ml[0][0];
#pragma unroll
      for (int i = 0; i < 4; ++i) M = __builtin_fmaxf(M, __builtin_fmaxf(ml[i][0], ml[i][2]));
      float L = 0.f; f32x2 acc = {0.f, 0.f};
#pragma unroll
      for (int w = 0; w < 8; ++w) {
        const float f = __builtin_amdgcn_exp2f(ml[w >> 1][(w & 1) * 2] - M);
        L = __builtin_fmaf(f, ml[w >> 1][(w & 1) * 2 + 1], L);
        acc = __builtin_elementwise_fma(av[w], f32x2{f, f}, acc);
      }
      const float ax = acc[0], ay = acc[1];
      __builtin_amdgcn_raw_buffer_store_b128(u32x4{f2u(ax), tag, f2u(ay), tag}, rw, t * 16, WS_H1 + b * 4096, 16);
      if ((t & 127) == 0) __builtin_amdgcn_raw_buffer_store_b128(u32x4{f2u(M), tag, f2u(L), tag}, rw, h * 16, WS_ML + b * 64, 16);
      TS(6);
    } else if (wave == 4 && grp < 28) {
      const int ci = ub / 56, cl = ub % 56;
      const unsigned xv = x32[cl * 4 + ci];
      u32x4 g[4];
#pragma unroll 1
      for (int spin = 0; spin < SPIN_MAX; ++spin) {
        bool ok = true;
#pragma unroll
        for (int k = 0; k < 4; ++k) g[k] = __builtin_amdgcn_raw_buffer_load_b128(rw, lane * 4096, WS_H2 + k * 262144 + ci * 1024 + cl * 16, 16);
#pragma unroll
        for (int k = 0; k < 4; ++k) ok &= (g[k][1] == tag) & (g[k][3] == tag);
        if (__builtin_amdgcn_ballot_w64(!ok) == 0) break;
        __builtin_amdgcn_s_sleep(2);
      }
      TS(6);
      float s0 = 0.f, s1 = 0.f;
#pragma unroll
      for (int k = 0; k < 4; ++k) { unsigned g0 = g[k][0], g2 = g[k][2]; s0 += u2f(g0); s1 += u2f(g2); }
      float s = sw32_add(s0, s1);
      s += dpp<XOR1>(s); s += dpp<XOR2>(s); s += dpp<XOR7>(s); s += dpp<ROR8>(s); s = sw16_add(s, s);
      const float o0 = rdl(s, 0) + u2f(xv << 16), o1 = rdl(s, 32) + u2f(xv & 0xffff0000u);
      if (lane == 0) {
        out32[cl * 4 + ci] = pkbf(o0, o1);
        if (ub == 0) __builtin_amdgcn_raw_buffer_store_b32(tag, rw, 0, WS_EPOCH, 16);
      }
      TS(7);
    }
    asm volatile("" :: "v"(kb[NBACK][0]), "v"(kb[NBACK][1]), "v"(vb[NBACK][0]), "v"(vb[NBACK][1]));
  } else {
    __builtin_amdgcn_s_setprio(3);
    const int g = b >> 5;
    const unsigned ep = __builtin_amdgcn_raw_buffer_load_b32(rw, 0, WS_EPOCH, 16);
    unsigned xr[7];
#pragma unroll
    for (int j = 0; j < 7; ++j) xr[j] = x32[(lane >> 1) + 32 * j];
    u32x4 w[14];
    const int jq = mem * 4 + (grp & 3);
    {
      const rsrc_t r = __builtin_amdgcn_make_buffer_rsrc((void*)wq, 0, 448 * 8192, 0x00020000);
      const int vo = (lane >> 1) * 16384 + (lane & 1) * 16;
#pragma unroll
      for (int i = 0; i < 14; ++i) w[i] = __builtin_amdgcn_raw_buffer_load_b128(r, vo, g * 1024 + jq * 32 + (i >> 1) * 524288 + (i & 1) * 8192, WQ_AUX);
    }
    if (mem == 0 && lane == 0) __builtin_amdgcn_raw_buffer_store_b32(0u, rw, 0, WS_CTR + grp * 256, 16);
    SCHED();
#pragma unroll
    for (int i = 0; i < 4; ++i) { const int k = lane + 64 * i; ids[k] = k < S_STATIC ? k * 8 + mem : -1; }
    if (lane < 8) ids[256 + lane] = -1;
    if (lane < 4) ctl[lane] = 0;
    asm volatile("s_waitcnt lgkmcnt(0)\n s_barrier" ::: "memory");
    asm volatile("" : "+v"(xr[0]), "+v"(xr[1]), "+v"(xr[2]), "+v"(xr[3]), "+v"(xr[4]), "+v"(xr[5]), "+v"(xr[6]));
    unsigned xp[7];
    {
      float s = 0.f;
#pragma unroll
      for (int j = 0; j < 7; ++j) s += u2f(xr[j] << 16) + u2f(xr[j] & 0xffff0000u);
      const float mean = wave_sum(s) * (1.f / 896.f);
      float v = 0.f;
#pragma unroll
      for (int j = 0; j < 7; ++j) { const float da = u2f(xr[j] << 16) - mean, db = u2f(xr[j] & 0xffff0000u) - mean; v += da * da + db * db; }
      const float var = wave_sum(v) * (1.f / 896.f);
      const float mb = rbf(mean), rden = __builtin_amdgcn_rcpf(rbf(__builtin_amdgcn_sqrtf(rbf(rbf(var) + 1e-5f))));
#pragma unroll
      for (int j = 0; j < 7; ++j) xp[j] = pkbf(rbf(u2f(xr[j] << 16) - mb) * rden, rbf(u2f(xr[j] & 0xffff0000u) - mb) * rden);
    }
    tag = __builtin_amdgcn_readfirstlane(ep) + 1u;
    TS(1);
    {
      float r0, r1;
      gemv16(w, xp, r0, r1);
      const unsigned pk = pkbf(r0, r1);
      auto s = __builtin_amdgcn_permlane16_swap(pk, pk, false, false);
      unsigned s0 = s[0], s1 = s[1];
      asm volatile("s_waitcnt vmcnt(0)" : "+v"(s0), "+v"(s1) :: "memory");
      if ((lane & 30) == 0)
        __builtin_amdgcn_raw_buffer_store_b128(u32x4{s0, tag, s1, tag}, rw, ((lane & 1) * 2 + (lane >> 5)) * 16, WS_Q + g * 2048 + jq * 64, 16);
    }
    TS(2);
    {
      u32x4 g0, g1;
#pragma unroll 1
      for (int spin = 0; spin < SPIN_MAX; ++spin) {
        g0 = __builtin_amdgcn_raw_buffer_load_b128(rw, lane * 16 + g * 2048 + WS_Q, 0, 16);
        g1 = __builtin_amdgcn_raw_buffer_load_b128(rw, lane * 16 + g * 2048 + WS_Q + 1024, 0, 16);
        const bool ok = (g0[1] == tag) & (g0[3] == tag) & (g1[1] == tag) & (g1[3] == tag);
        if (__builtin_amdgcn_ballot_w64(!ok) == 0) break;
        __builtin_amdgcn_s_sleep(1);
      }
      *(u32x2*)&qs[lane * 2] = u32x2{g0[0], g0[2]};
      *(u32x2*)&qs[128 + lane * 2] = u32x2{g1[0], g1[2]};
      if (lane == 0) STL(&ctl[2], (int)tag);
      asm volatile("s_waitcnt lgkmcnt(0)" ::: "memory");
      if (lane == 0) STL(&ctl[1], 1);
    }
    TS(3);
    float fo[8];
    unsigned wp[8][8];
    auto tail_weights = [&]() {
#pragma unroll
      for (int i = 0; i < 8; ++i) fo[i] = 0.f;
      if (grp < 28) {
        const char* base = ((lane & 1) ? wu : wg) + (lane >> 1) * 7168 + ub * 16;
#pragma unroll
        for (int i = 0; i < 14; ++i) {
          const u32x4* wptr = (const u32x4*)(base + (i >> 1) * 229376 + (i & 1) * 3584);
          w[i] = GU_NT ? __builtin_nontemporal_load(wptr) : *wptr;
        }
        const rsrc_t r = __builtin_amdgcn_make_buffer_rsrc((void*)wd, 0, 1792 * 896, 0x00020000);
        u32x4 dv[8];
#pragma unroll
        for (int i = 0; i < 8; ++i) dv[i] = __builtin_amdgcn_raw_buffer_load_b128(r, lane * 16, (ub * 8 + i) * 896, WO_AUX);
        float r0, r1;
        gemv16(w, xp, r0, r1);
        r0 = rbf(r0); r1 = rbf(r1);
        const float u0 = dpp<XOR1>(r0), u1 = dpp<XOR1>(r1);
        const float s0 = rbf(r0 * __builtin_amdgcn_rcpf(1.f + __expf(-r0))), s1 = rbf(r1 * __builtin_amdgcn_rcpf(1.f + __expf(-r1)));
        const unsigned hp = pkbf(s0 * u0, s1 * u1);
#pragma unroll
        for (int pc = 0; pc < 4; ++pc) {
          const unsigned hs = __builtin_amdgcn_readlane(hp, pc * 16);
#pragma unroll
          for (int k = 0; k < 4; ++k) {
            unsigned w0 = dv[2 * pc][k], w1 = dv[2 * pc + 1][k];
            fo[2 * k] = dot2(__builtin_amdgcn_perm(w1, w0, 0x05040100u), hs, fo[2 * k]);
            fo[2 * k + 1] = dot2(__builtin_amdgcn_perm(w1, w0, 0x07060302u), hs, fo[2 * k + 1]);
          }
        }
      }
      SCHED();
      const rsrc_t r = __builtin_amdgcn_make_buffer_rsrc((void*)wo, 0, 4096 * 896, 0x00020000);
      u32x4 ov[16];
#pragma unroll
      for (int i = 0; i < 16; ++i) ov[i] = __builtin_amdgcn_raw_buffer_load_b128(r, lane * 16, (b * 16 + i) * 896, WO_AUX);
#pragma unroll
      for (int pc = 0; pc < 8; ++pc)
#pragma unroll
        for (int k = 0; k < 4; ++k) {
          unsigned w0 = ov[2 * pc][k], w1 = ov[2 * pc + 1][k];
          wp[pc][2 * k] = __builtin_amdgcn_perm(w1, w0, 0x05040100u);
          wp[pc][2 * k + 1] = __builtin_amdgcn_perm(w1, w0, 0x07060302u);
          asm volatile("" : "+v"(wp[pc][2 * k]), "+v"(wp[pc][2 * k + 1]));
        }
      SCHED();
    };
    if (!LATE_W) tail_weights();
    TS(4);
    {
      const rsrc_t rc = __builtin_amdgcn_make_buffer_rsrc((void*)(ws + WS_CTR + grp * 256), 0, 4, 0x00020000);
      int k = S_STATIC;
#pragma unroll 1
      for (int spin = 0; spin < 16 * SPIN_MAX && k < 260; ++spin) {
        const int ld = __builtin_amdgcn_readfirstlane(LDL(&ctl[0]));
        if (k <= ld + AHEAD) {
          const int p = __builtin_amdgcn_raw_ptr_buffer_atomic_add_i32(1, rc, lane * 4, 0, 16);
          const unsigned id = (unsigned)__builtin_amdgcn_readfirstlane(p) + S_STATIC * 8;
          const int idc = id < (unsigned)NCH ? (int)id : NCH;
          if (lane == 0) STL(&ids[k], idc);
          ++k;
          if (idc >= NCH) break;
        } else {
          __builtin_amdgcn_s_sleep(2);
        }
      }
    }
    TS(5);
    if (LATE_W) tail_weights();
    asm volatile("s_waitcnt lgkmcnt(0)\n s_barrier" ::: "memory");
    TS(6);
    {
      const int j1 = b & 31;
      u32x4 ga[4], gm[4];
      const int vo = (lane >> 3) * 4096 + (lane & 7) * 16, vm = (lane >> 3) * 64;
#pragma unroll 1
      for (int spin = 0; spin < SPIN_MAX; ++spin) {
#pragma unroll
        for (int i = 0; i < 4; ++i) {
          ga[i] = __builtin_amdgcn_raw_buffer_load_b128(rw, vo, WS_H1 + (g * 32 + i * 8) * 4096 + j1 * 128, 16);
          gm[i] = __builtin_amdgcn_raw_buffer_load_b128(rw, vm, WS_ML + (g * 32 + i * 8) * 64 + (j1 >> 4) * 16, 16);
        }
        bool ok = true;
#pragma unroll
        for (int i = 0; i < 4; ++i) ok &= (ga[i][1] == tag) & (ga[i][3] == tag) & (gm[i][1] == tag) & (gm[i][3] == tag);
        if (__builtin_amdgcn_ballot_w64(!ok) == 0) break;
        __builtin_amdgcn_s_sleep(1);
      }
      TS(7);
      float mv[4];
#pragma unroll
      for (int i = 0; i < 4; ++i) { unsigned t0 = gm[i][0]; mv[i] = u2f(t0); }
      float M = __builtin_fmaxf(__builtin_fmaxf(mv[0], mv[1]), __builtin_fmaxf(mv[2], mv[3]));
      M = __builtin_fmaxf(M, dpp<ROR8>(M)); M = sw16_max(M); M = sw32_max(M);
      float L = 0.f, s0 = 0.f, s1 = 0.f;
#pragma unroll
      for (int i = 0; i < 4; ++i) {
        const float f = __builtin_amdgcn_exp2f(mv[i] - M);
        unsigned t0 = ga[i][0], t1 = ga[i][2], t2 = gm[i][2];
        s0 = __builtin_fmaf(f, u2f(t0), s0); s1 = __builtin_fmaf(f, u2f(t1), s1); L = __builtin_fmaf(f, u2f(t2), L);
      }
      s0 += dpp<ROR8>(s0); s1 += dpp<ROR8>(s1); L += dpp<ROR8>(L);
      s0 = sw16_add(s0, s0); s1 = sw16_add(s1, s1); L = sw16_add(L, L);
      s0 = sw32_add(s0, s0); s1 = sw32_add(s1, s1); L = sw32_add(L, L);
      const float rl = __builtin_amdgcn_rcpf(L);
      const unsigned op = pkbf(s0 * rl, s1 * rl);
#pragma unroll
      for (int pc = 0; pc < 8; ++pc) {
        const unsigned os = __builtin_amdgcn_readlane(op, pc);
#pragma unroll
        for (int c = 0; c < 8; ++c) fo[c] = dot2(wp[pc][c], os, fo[c]);
      }
      if (lane < 56) {
#pragma unroll
        for (int i = 0; i < 4; ++i)
          __builtin_amdgcn_raw_buffer_store_b128(u32x4{f2u(fo[2 * i]), tag, f2u(fo[2 * i + 1]), tag}, rw, lane * 16, WS_H2 + b * 4096 + i * 1024, 16);
      }
      TS(8);
    }
  }
}

extern "C" void kernel_launch(void* const* d_in, const int* in_sizes, int n_in, void* d_out, int out_size, void* d_ws,
                              size_t ws_size, hipStream_t stream) {
  fused_decode<<<256, 576, 0, stream>>>((const unsigned*)d_in[0], (const char*)d_in[1], (const char*)d_in[2],
                                        (const char*)d_in[3], (const char*)d_in[4], (const char*)d_in[6],
                                        (const char*)d_in[7], (const char*)d_in[8], (unsigned*)d_out, (char*)d_ws);
}
```

```cpp
#include <hip/hip_runtime.h>
#include <stdint.h>

typedef unsigned u32x4 __attribute__((ext_vector_type(4)));
typedef unsigned u32x2 __attribute__((ext_vector_type(2)));
typedef float f32x2 __attribute__((ext_vector_type(2)));
typedef float f32x4 __attribute__((ext_vector_type(4)));
typedef __bf16 bf16x2 __attribute__((ext_vector_type(2)));
#define LAS __attribute__((address_space(3)))
typedef __amdgpu_buffer_rsrc_t rsrc_t;

#define S_STATIC 24
#define AHEAD 2
#define NBACK 6
#define ROT 0x100000u
#define WQ_AUX 2
#define BAR_FIRST 1
#define GU_NT 1
#define MAP 0
#define K_AUX 2
#define V_AUX 2
#define WO_AUX 2
#define TLBW 0
#define LATE_W 0

constexpr unsigned REG = 4u << 20;
constexpr int NCH = 256;
constexpr unsigned OOB = 0x40000000u;
constexpr int SPIN_MAX = 200000;
constexpr unsigned WS_EPOCH = 0, WS_CTR = 4096, WS_Q = 16384, WS_ML = 32768, WS_H1 = 65536, WS_H2 = 65536 + (1u << 20);
constexpr unsigned WS_USED = WS_H2 + (1u << 20);
constexpr float CSC = 0.0625f * 1.4426950408889634f;

__device__ __forceinline__ float u2f(unsigned u) { return __builtin_bit_cast(float, u); }
__device__ __forceinline__ unsigned f2u(float f) { return __builtin_bit_cast(unsigned, f); }
__device__ __forceinline__ float rbf(float f) { __bf16 b = (__bf16)f; return (float)b; }
__device__ __forceinline__ unsigned pkbf(float lo, float hi) {
  bf16x2 v; v[0] = (__bf16)lo; v[1] = (__bf16)hi; return __builtin_bit_cast(unsigned, v);
}
__device__ __forceinline__ float dot2(unsigned a, unsigned b, float acc) {
  return __builtin_amdgcn_fdot2_f32_bf16(__builtin_bit_cast(bf16x2, a), __builtin_bit_cast(bf16x2, b), acc, false);
}
template <int CTRL> __device__ __forceinline__ float dpp(float x) {
  return __builtin_bit_cast(float, __builtin_amdgcn_mov_dpp(__builtin_bit_cast(int, x), CTRL, 0xf, 0xf, true));
}
constexpr int XOR1 = 0xB1, XOR2 = 0x4E, XOR7 = 0x141, ROR8 = 0x128, ROR4 = 0x124, BC0 = 0x150, BC8 = 0x158;
__device__ __forceinline__ float sw16_add(float a, float b) {
  auto s = __builtin_amdgcn_permlane16_swap(f2u(a), f2u(b), false, false);
  unsigned s0 = s[0], s1 = s[1]; return u2f(s0) + u2f(s1);
}
__device__ __forceinline__ float sw32_add(float a, float b) {
  auto s = __builtin_amdgcn_permlane32_swap(f2u(a), f2u(b), false, false);
  unsigned s0 = s[0], s1 = s[1]; return u2f(s0) + u2f(s1);
}
__device__ __forceinline__ float sw16_max(float a) {
  auto s = __builtin_amdgcn_permlane16_swap(f2u(a), f2u(a), false, false);
  unsigned s0 = s[0], s1 = s[1]; return __builtin_fmaxf(u2f(s0), u2f(s1));
}
__device__ __forceinline__ float sw32_max(float a) {
  auto s = __builtin_amdgcn_permlane32_swap(f2u(a), f2u(a), false, false);
  unsigned s0 = s[0], s1 = s[1]; return __builtin_fmaxf(u2f(s0), u2f(s1));
}
__device__ __forceinline__ float wave_sum(float t) {
  t += dpp<XOR1>(t); t += dpp<XOR2>(t); t += dpp<XOR7>(t); t += dpp<ROR8>(t);
  t = sw16_add(t, t); return sw32_add(t, t);
}
__device__ __forceinline__ float rdl(float v, int l) { return u2f(__builtin_amdgcn_readlane(f2u(v), l)); }
#define LDL(p) __hip_atomic_load((LAS int*)(p), __ATOMIC_RELAXED, __HIP_MEMORY_SCOPE_WORKGROUP)
#define STL(p, v) __hip_atomic_store((LAS int*)(p), v, __ATOMIC_RELAXED, __HIP_MEMORY_SCOPE_WORKGROUP)
#define SCHED() __builtin_amdgcn_sched_barrier(0)

#define TS(i) do {} while (0)

template <bool REFILL>
__device__ __forceinline__ void step(u32x4 (&kr)[2], u32x4 (&vr)[2], const u32x4& q0, const u32x4& q1, f32x2 (&a0)[4],
                                     f32x2 (&a1)[4], float& m, float& mc, float& l, rsrc_t rk, rsrc_t rv, int voff,
                                     unsigned o, bool lane8) {
  float d[4];
#pragma unroll
  for (int u = 0; u < 2; ++u) {
    const u32x4 k = kr[u];
    float x = 0.f, y = 0.f;
#pragma unroll
    for (int e = 0; e < 4; ++e) { unsigned w = k[e], qa = q0[e], qb = q1[e]; x = dot2(w, qa, x); y = dot2(w, qb, y); }
    if (REFILL) {
      asm volatile("" : "+v"(x), "+v"(y) :: "memory");
      kr[u] = __builtin_amdgcn_raw_buffer_load_b128(rk, voff + u * 1024, o, K_AUX);
      SCHED();
    }
    d[2 * u] = x; d[2 * u + 1] = y;
  }
  float ab = sw16_add(d[0], d[1]), cd = sw16_add(d[2], d[3]);
  ab += dpp<ROR8>(ab); cd += dpp<ROR8>(cd);
  float t = lane8 ? cd : ab;
  t += dpp<XOR7>(t); t += dpp<XOR2>(t); t += dpp<XOR1>(t);
  t = rbf(t);
  if (__builtin_amdgcn_ballot_w64(t > m) != 0) {
    float mx = __builtin_fmaxf(t, dpp<ROR8>(t));
    mx = __builtin_fmaxf(sw32_max(mx), m);
    const float mcn = mx * CSC;
    const float f = __builtin_amdgcn_exp2f(mc - mcn);
    m = mx; mc = mcn; l *= f;
    const float f0 = rdl(f, 0), f1 = rdl(f, 16);
#pragma unroll
    for (int k = 0; k < 4; ++k) { a0[k] *= f0; a1[k] *= f1; }
  }
  const float p = __builtin_amdgcn_exp2f(__builtin_fmaf(t, CSC, -mc));
  l += p;
  auto s = __builtin_amdgcn_permlane16_swap(f2u(p), f2u(p), false, false);
  unsigned s0 = s[0], s1 = s[1];
  const float ph0 = u2f(s0), ph1 = u2f(s1);
  const float pp[4] = {dpp<BC0>(ph0), dpp<BC0>(ph1), dpp<BC8>(ph0), dpp<BC8>(ph1)};
#pragma unroll
  for (int u = 0; u < 2; ++u) {
    const u32x4 v = vr[u];
    const f32x2 p0 = {pp[2 * u], pp[2 * u]}, p1 = {pp[2 * u + 1], pp[2 * u + 1]};
#pragma unroll
    for (int e = 0; e < 4; ++e) {
      unsigned w = v[e];
      const f32x2 x = {u2f(w << 16), u2f(w & 0xffff0000u)};
      a0[e] = __builtin_elementwise_fma(x, p0, a0[e]);
      a1[e] = __builtin_elementwise_fma(x, p1, a1[e]);
    }
    if (REFILL) {
      asm volatile("" : "+v"(a0[0]), "+v"(a0[1]), "+v"(a0[2]), "+v"(a0[3]), "+v"(a1[0]), "+v"(a1[1]), "+v"(a1[2]), "+v"(a1[3]) :: "memory");
      vr[u] = __builtin_amdgcn_raw_buffer_load_b128(rv, voff + u * 1024, o, V_AUX);
      SCHED();
    }
  }
}

__device__ __forceinline__ void gemv16(const u32x4 (&w)[14], const unsigned (&xp)[7], float& r0, float& r1) {
  float a[8];
#pragma unroll
  for (int i = 0; i < 8; ++i) a[i] = 0.f;
#pragma unroll
  for (int j = 0; j < 7; ++j) {
#pragma unroll
    for (int k = 0; k < 4; ++k) {
      unsigned w0 = w[2 * j][k], w1 = w[2 * j + 1][k];
      a[2 * k] = dot2(__builtin_amdgcn_perm(w1, w0, 0x05040100u), xp[j], a[2 * k]);
      a[2 * k + 1] = dot2(__builtin_amdgcn_perm(w1, w0, 0x07060302u), xp[j], a[2 * k + 1]);
    }
  }
  float b[4];
#pragma unroll
  for (int i = 0; i < 4; ++i) b[i] = sw32_add(a[i], a[i + 4]);
  r0 = sw16_add(b[0], b[2]); r1 = sw16_add(b[1], b[3]);
  r0 += dpp<ROR8>(r0); r1 += dpp<ROR8>(r1);
  r0 += dpp<ROR4>(r0); r1 += dpp<ROR4>(r1);
  r0 += dpp<XOR2>(r0); r1 += dpp<XOR2>(r1);
}

__global__ void __launch_bounds__(576) fused_decode(const unsigned* __restrict__ x32, const char* __restrict__ K,
                                                    const char* __restrict__ V, const char* __restrict__ wq,
                                                    const char* __restrict__ wo, const char* __restrict__ wg,
                                                    const char* __restrict__ wu, const char* __restrict__ wd,
                                                    unsigned* __restrict__ out32, char* ws) {
  __shared__ __attribute__((aligned(16))) float macc[8 * 512];
  __shared__ __attribute__((aligned(16))) unsigned qs[256];
  __shared__ __attribute__((aligned(16))) float mml[32];
  __shared__ int ids[264];
  __shared__ int ctl[4];
  const int lane = threadIdx.x & 63, hwave = __builtin_amdgcn_readfirstlane(threadIdx.x >> 6);
  const int wave = hwave == 0 ? 8 : hwave - 1;
  const int b = blockIdx.x, grp = b >> 3, mem = b & 7;
  const int ub = mem * 28 + grp;
  asm volatile("" :: "s"(x32), "s"(K), "s"(V), "s"(wq), "s"(wo), "s"(wg), "s"(wu), "s"(wd), "s"(out32), "s"(ws));
  TS(0);
  const rsrc_t rw = __builtin_amdgcn_make_buffer_rsrc((void*)ws, 0, WS_USED, 0x00020000);
  unsigned tag;

  if (__builtin_expect(wave < 8, 1)) {
    const rsrc_t rk = __builtin_amdgcn_make_buffer_rsrc((void*)(K + (size_t)grp * REG), 0, REG, 0x00020000);
    const rsrc_t rv = __builtin_amdgcn_make_buffer_rsrc((void*)(V + (size_t)grp * REG), 0, REG, 0x00020000);
    const int voff = lane * 16 + wave * ((MAP & 2) ? 4096 : 2048);
    const unsigned rot = grp * ROT;
    auto offs = [&](int id) -> unsigned {
      unsigned c = id;
      if (MAP == 1 || MAP == 3) c = id < S_STATIC * 8 ? (id & 7) * S_STATIC + (id >> 3) : id;
      if (MAP == 2) c = id < S_STATIC * 8 ? (id >> 4) * 16 + (id & 7) * 2 + ((id >> 3) & 1) : id;
      const unsigned o = (MAP & 2) ? (c >> 1) * 32768u + (c & 1) * 2048u : c * 16384u;
      return id < NCH ? ((o + rot) & (REG - 1)) : OOB;
    };
    u32x4 kb[NBACK + 1][2], vb[NBACK + 1][2];
    if (TLBW == 2 && wave == 7) {
      unsigned t0 = __builtin_amdgcn_raw_buffer_load_b32(rk, lane * 65536, 0, 2);
      unsigned t1 = __builtin_amdgcn_raw_buffer_load_b32(rv, lane * 65536, 0, 2);
      unsigned t2 = __builtin_amdgcn_raw_buffer_load_b32(rw, lane * 65536, 0, 2);
      unsigned t3 = __builtin_nontemporal_load(out32);
      asm volatile("" :: "v"(t0), "v"(t1), "v"(t2), "v"(t3));
    }
    if (TLBW == 3 && wave == 7) {
      unsigned t0 = __builtin_amdgcn_raw_buffer_load_b32(rk, (lane & 3) * 1048576, 0, 2);
      unsigned t1 = __builtin_amdgcn_raw_buffer_load_b32(rv, (lane & 3) * 1048576, 0, 2);
      unsigned t2 = __builtin_amdgcn_raw_buffer_load_b32(rw, (lane & 3) * 1048576, 0, 2);
      unsigned t3 = __builtin_nontemporal_load(out32);
      asm volatile("" :: "v"(t0), "v"(t1), "v"(t2), "v"(t3));
    }
    if (BAR_FIRST) { asm volatile("s_barrier" ::: "memory"); SCHED(); }
#pragma unroll
    for (int s = 0; s <= NBACK; ++s) {
      const unsigned o = offs(s * 8 + mem);
#pragma unroll
      for (int u = 0; u < 2; ++u) {
        if (s > 0) asm volatile("s_waitcnt vmcnt(3)" ::: "memory");
        kb[s][u] = __builtin_amdgcn_raw_buffer_load_b128(rk, voff + u * 1024, o, K_AUX);
        SCHED();
      }
#pragma unroll
      for (int u = 0; u < 2; ++u) {
        if (s > 0) asm volatile("s_waitcnt vmcnt(3)" ::: "memory");
        vb[s][u] = __builtin_amdgcn_raw_buffer_load_b128(rv, voff + u * 1024, o, V_AUX);
        SCHED();
      }
      if (!BAR_FIRST && s == 0) { asm volatile("s_barrier" ::: "memory"); SCHED(); }
    }
    TS(1);
    {
      int spin = 0;
#pragma unroll 1
      while (__builtin_amdgcn_readfirstlane(LDL(&ctl[1])) == 0 && ++spin < SPIN_MAX) __builtin_amdgcn_s_sleep(1);
    }
    asm volatile("" ::: "memory");
    TS(2);
    const u32x4 q0 = *(const u32x4*)&qs[(lane & 31) * 4], q1 = *(const u32x4*)&qs[128 + (lane & 31) * 4];
    tag = __builtin_amdgcn_readfirstlane(LDL(&ctl[2]));
    f32x2 a0[4], a1[4];
#pragma unroll
    for (int k = 0; k < 4; ++k) { a0[k] = f32x2{0.f, 0.f}; a1[k] = f32x2{0.f, 0.f}; }
    float m = -INFINITY, mc = -INFINITY, l = 0.f;
    const bool lane8 = (lane & 8) != 0;
#pragma unroll
    for (int s = 0; s < NBACK; ++s) {
      step<true>(kb[NBACK], vb[NBACK], q0, q1, a0, a1, m, mc, l, rk, rv, voff, offs((NBACK + 1 + s) * 8 + mem), lane8);
      step<false>(kb[s], vb[s], q0, q1, a0, a1, m, mc, l, rk, rv, voff, 0, lane8);
      SCHED();
    }
    TS(3);
    int n = 2 * NBACK, nxt = (2 * NBACK + 1) * 8 + mem;
#pragma unroll 1
    for (;;) {
      if (lane == 0) __hip_atomic_fetch_max(&ctl[0], n, __ATOMIC_RELAXED, __HIP_MEMORY_SCOPE_WORKGROUP);
      step<true>(kb[NBACK], vb[NBACK], q0, q1, a0, a1, m, mc, l, rk, rv, voff, offs(nxt), lane8);
      if (nxt >= NCH) break;
      int id = __builtin_amdgcn_readfirstlane(LDL(&ids[n + 2]));
      int spin = 0;
#pragma unroll 1
      while (id < 0 && ++spin < SPIN_MAX) { __builtin_amdgcn_s_sleep(1); id = __builtin_amdgcn_readfirstlane(LDL(&ids[n + 2])); }
      nxt = id < 0 ? NCH : id;
      ++n;
    }
    TS(4);
    f32x4 lo, hi;
    lo[0] = sw32_add(a0[0][0], a1[0][0]); lo[1] = sw32_add(a0[0][1], a1[0][1]);
    lo[2] = sw32_add(a0[1][0], a1[1][0]); lo[3] = sw32_add(a0[1][1], a1[1][1]);
    hi[0] = sw32_add(a0[2][0], a1[2][0]); hi[1] = sw32_add(a0[2][1], a1[2][1]);
    hi[2] = sw32_add(a0[3][0], a1[3][0]); hi[3] = sw32_add(a0[3][1], a1[3][1]);
    *(f32x4*)&macc[wave * 512 + lane * 8] = lo;
    *(f32x4*)&macc[wave * 512 + lane * 8 + 4] = hi;
    l += dpp<ROR8>(l); l = sw32_add(l, l);
    if ((lane & 47) == 0) *(f32x2*)&mml[(lane >> 4) * 16 + wave * 2] = f32x2{mc, l};
    asm volatile("s_waitcnt lgkmcnt(0)\n s_barrier" ::: "memory");
    TS(5);
    if (wave < 4) {
      const int t = wave * 64 + lane, h = wave >> 1;
      f32x4 ml[4]; f32x2 av[8];
#pragma unroll
      for (int i = 0; i < 4; ++i) ml[i] = *(const f32x4*)&mml[h * 16 + i * 4];
#pragma unroll
      for (int w = 0; w < 8; ++w) av[w] = *(const f32x2*)&macc[w * 512 + 2 * t];
      float M = ml[0][0];
#pragma unroll
      for (int i = 0; i < 4; ++i) M = __builtin_fmaxf(M, __builtin_fmaxf(ml[i][0], ml[i][2]));
      float L = 0.f; f32x2 acc = {0.f, 0.f};
#pragma unroll
      for (int w = 0; w < 8; ++w) {
        const float f = __builtin_amdgcn_exp2f(ml[w >> 1][(w & 1) * 2] - M);
        L = __builtin_fmaf(f, ml[w >> 1][(w & 1) * 2 + 1], L);
        acc = __builtin_elementwise_fma(av[w], f32x2{f, f}, acc);
      }
      const float ax = acc[0], ay = acc[1];
      __builtin_amdgcn_raw_buffer_store_b128(u32x4{f2u(ax), tag, f2u(ay), tag}, rw, t * 16, WS_H1 + b * 4096, 16);
      if ((t & 127) == 0) __builtin_amdgcn_raw_buffer_store_b128(u32x4{f2u(M), tag, f2u(L), tag}, rw, h * 16, WS_ML + b * 64, 16);
      TS(6);
    } else if (wave == 4 && grp < 28) {
      const int ci = ub / 56, cl = ub % 56;
      const unsigned xv = x32[cl * 4 + ci];
      u32x4 g[4];
#pragma unroll 1
      for (int spin = 0; spin < SPIN_MAX; ++spin) {
        bool ok = true;
#pragma unroll
        for (int k = 0; k < 4; ++k) g[k] = __builtin_amdgcn_raw_buffer_load_b128(rw, lane * 4096, WS_H2 + k * 262144 + ci * 1024 + cl * 16, 16);
#pragma unroll
        for (int k = 0; k < 4; ++k) ok &= (g[k][1] == tag) & (g[k][3] == tag);
        if (__builtin_amdgcn_ballot_w64(!ok) == 0) break;
        __builtin_amdgcn_s_sleep(2);
      }
      TS(6);
      float s0 = 0.f, s1 = 0.f;
#pragma unroll
      for (int k = 0; k < 4; ++k) { unsigned g0 = g[k][0], g2 = g[k][2]; s0 += u2f(g0); s1 += u2f(g2); }
      float s = sw32_add(s0, s1);
      s += dpp<XOR1>(s); s += dpp<XOR2>(s); s += dpp<XOR7>(s); s += dpp<ROR8>(s); s = sw16_add(s, s);
      const float o0 = rdl(s, 0) + u2f(xv << 16), o1 = rdl(s, 32) + u2f(xv & 0xffff0000u);
      if (lane == 0) {
        out32[cl * 4 + ci] = pkbf(o0, o1);
        if (ub == 0) __builtin_amdgcn_raw_buffer_store_b32(tag, rw, 0, WS_EPOCH, 16);
      }
      TS(7);
    }
    asm volatile("" :: "v"(kb[NBACK][0]), "v"(kb[NBACK][1]), "v"(vb[NBACK][0]), "v"(vb[NBACK][1]));
  } else {
    __builtin_amdgcn_s_setprio(3);
    const int g = b >> 5;
    const unsigned ep = __builtin_amdgcn_raw_buffer_load_b32(rw, 0, WS_EPOCH, 16);
    unsigned xr[7];
#pragma unroll
    for (int j = 0; j < 7; ++j) xr[j] = x32[(lane >> 1) + 32 * j];
    u32x4 w[14];
    const int jq = mem * 4 + (grp & 3);
    {
      const rsrc_t r = __builtin_amdgcn_make_buffer_rsrc((void*)wq, 0, 448 * 8192, 0x00020000);
      const int vo = (lane >> 1) * 16384 + (lane & 1) * 16;
#pragma unroll
      for (int i = 0; i < 14; ++i) w[i] = __builtin_amdgcn_raw_buffer_load_b128(r, vo, g * 1024 + jq * 32 + (i >> 1) * 524288 + (i & 1) * 8192, WQ_AUX);
    }
    if (mem == 0 && lane == 0) __builtin_amdgcn_raw_buffer_store_b32(0u, rw, 0, WS_CTR + grp * 256, 16);
    SCHED();
#pragma unroll
    for (int i = 0; i < 4; ++i) { const int k = lane + 64 * i; ids[k] = k < S_STATIC ? k * 8 + mem : -1; }
    if (lane < 8) ids[256 + lane] = -1;
    if (lane < 4) ctl[lane] = 0;
    asm volatile("s_waitcnt lgkmcnt(0)\n s_barrier" ::: "memory");
    asm volatile("" : "+v"(xr[0]), "+v"(xr[1]), "+v"(xr[2]), "+v"(xr[3]), "+v"(xr[4]), "+v"(xr[5]), "+v"(xr[6]));
    unsigned xp[7];
    {
      float s = 0.f;
#pragma unroll
      for (int j = 0; j < 7; ++j) s += u2f(xr[j] << 16) + u2f(xr[j] & 0xffff0000u);
      const float mean = wave_sum(s) * (1.f / 896.f);
      float v = 0.f;
#pragma unroll
      for (int j = 0; j < 7; ++j) { const float da = u2f(xr[j] << 16) - mean, db = u2f(xr[j] & 0xffff0000u) - mean; v += da * da + db * db; }
      const float var = wave_sum(v) * (1.f / 896.f);
      const float mb = rbf(mean), rden = __builtin_amdgcn_rcpf(rbf(__builtin_amdgcn_sqrtf(rbf(rbf(var) + 1e-5f))));
#pragma unroll
      for (int j = 0; j < 7; ++j) xp[j] = pkbf(rbf(u2f(xr[j] << 16) - mb) * rden, rbf(u2f(xr[j] & 0xffff0000u) - mb) * rden);
    }
    tag = __builtin_amdgcn_readfirstlane(ep) + 1u;
    TS(1);
    {
      float r0, r1;
      gemv16(w, xp, r0, r1);
      const unsigned pk = pkbf(r0, r1);
      auto s = __builtin_amdgcn_permlane16_swap(pk, pk, false, false);
      unsigned s0 = s[0], s1 = s[1];
      asm volatile("s_waitcnt vmcnt(0)" : "+v"(s0), "+v"(s1) :: "memory");
      if ((lane & 30) == 0)
        __builtin_amdgcn_raw_buffer_store_b128(u32x4{s0, tag, s1, tag}, rw, ((lane & 1) * 2 + (lane >> 5)) * 16, WS_Q + g * 2048 + jq * 64, 16);
    }
    TS(2);
    {
      u32x4 g0, g1;
#pragma unroll 1
      for (int spin = 0; spin < SPIN_MAX; ++spin) {
        g0 = __builtin_amdgcn_raw_buffer_load_b128(rw, lane * 16 + g * 2048 + WS_Q, 0, 16);
        g1 = __builtin_amdgcn_raw_buffer_load_b128(rw, lane * 16 + g * 2048 + WS_Q + 1024, 0, 16);
        const bool ok = (g0[1] == tag) & (g0[3] == tag) & (g1[1] == tag) & (g1[3] == tag);
        if (__builtin_amdgcn_ballot_w64(!ok) == 0) break;
        __builtin_amdgcn_s_sleep(1);
      }
      *(u32x2*)&qs[lane * 2] = u32x2{g0[0], g0[2]};
      *(u32x2*)&qs[128 + lane * 2] = u32x2{g1[0], g1[2]};
      if (lane == 0) STL(&ctl[2], (int)tag);
      asm volatile("s_waitcnt lgkmcnt(0)" ::: "memory");
      if (lane == 0) STL(&ctl[1], 1);
    }
    TS(3);
    if (TLBW == 1) {
      const rsrc_t rk = __builtin_amdgcn_make_buffer_rsrc((void*)(K + (size_t)grp * REG), 0, REG, 0x00020000);
      const rsrc_t rv = __builtin_amdgcn_make_buffer_rsrc((void*)(V + (size_t)grp * REG), 0, REG, 0x00020000);
      unsigned t0 = __builtin_amdgcn_raw_buffer_load_b32(rk, lane * 65536, 0, 2);
      unsigned t1 = __builtin_amdgcn_raw_buffer_load_b32(rv, lane * 65536, 0, 2);
      unsigned t2 = __builtin_amdgcn_raw_buffer_load_b32(rw, lane * 65536, 0, 2);
      unsigned t3 = __builtin_nontemporal_load(out32);
      asm volatile("" :: "v"(t0), "v"(t1), "v"(t2), "v"(t3));
    }
    float fo[8];
    unsigned wp[8][8];
    auto tail_weights = [&]() {
#pragma unroll
      for (int i = 0; i < 8; ++i) fo[i] = 0.f;
      if (grp < 28) {
        const char* base = ((lane & 1) ? wu : wg) + (lane >> 1) * 7168 + ub * 16;
#pragma unroll
        for (int i = 0; i < 14; ++i) {
          const u32x4* wptr = (const u32x4*)(base + (i >> 1) * 229376 + (i & 1) * 3584);
          w[i] = GU_NT ? __builtin_nontemporal_load(wptr) : *wptr;
        }
        const rsrc_t r = __builtin_amdgcn_make_buffer_rsrc((void*)wd, 0, 1792 * 896, 0x00020000);
        u32x4 dv[8];
#pragma unroll
        for (int i = 0; i < 8; ++i) dv[i] = __builtin_amdgcn_raw_buffer_load_b128(r, lane * 16, (ub * 8 + i) * 896, WO_AUX);
        float r0, r1;
        gemv16(w, xp, r0, r1);
        r0 = rbf(r0); r1 = rbf(r1);
        const float u0 = dpp<XOR1>(r0), u1 = dpp<XOR1>(r1);
        const float s0 = rbf(r0 * __builtin_amdgcn_rcpf(1.f + __expf(-r0))), s1 = rbf(r1 * __builtin_amdgcn_rcpf(1.f + __expf(-r1)));
        const unsigned hp = pkbf(s0 * u0, s1 * u1);
#pragma unroll
        for (int pc = 0; pc < 4; ++pc) {
          const unsigned hs = __builtin_amdgcn_readlane(hp, pc * 16);
#pragma unroll
          for (int k = 0; k < 4; ++k) {
            unsigned w0 = dv[2 * pc][k], w1 = dv[2 * pc + 1][k];
            fo[2 * k] = dot2(__builtin_amdgcn_perm(w1, w0, 0x05040100u), hs, fo[2 * k]);
            fo[2 * k + 1] = dot2(__builtin_amdgcn_perm(w1, w0, 0x07060302u), hs, fo[2 * k + 1]);
          }
        }
      }
      SCHED();
      const rsrc_t r = __builtin_amdgcn_make_buffer_rsrc((void*)wo, 0, 4096 * 896, 0x00020000);
      u32x4 ov[16];
#pragma unroll
      for (int i = 0; i < 16; ++i) ov[i] = __builtin_amdgcn_raw_buffer_load_b128(r, lane * 16, (b * 16 + i) * 896, WO_AUX);
#pragma unroll
      for (int pc = 0; pc < 8; ++pc)
#pragma unroll
        for (int k = 0; k < 4; ++k) {
          unsigned w0 = ov[2 * pc][k], w1 = ov[2 * pc + 1][k];
          wp[pc][2 * k] = __builtin_amdgcn_perm(w1, w0, 0x05040100u);
          wp[pc][2 * k + 1] = __builtin_amdgcn_perm(w1, w0, 0x07060302u);
          asm volatile("" : "+v"(wp[pc][2 * k]), "+v"(wp[pc][2 * k + 1]));
        }
      SCHED();
    };
    if (!LATE_W) tail_weights();
    TS(4);
    {
      const rsrc_t rc = __builtin_amdgcn_make_buffer_rsrc((void*)(ws + WS_CTR + grp * 256), 0, 4, 0x00020000);
      int k = S_STATIC;
#pragma unroll 1
      for (int spin = 0; spin < 16 * SPIN_MAX && k < 260; ++spin) {
        const int ld = __builtin_amdgcn_readfirstlane(LDL(&ctl[0]));
        if (k <= ld + AHEAD) {
          const int p = __builtin_amdgcn_raw_ptr_buffer_atomic_add_i32(1, rc, lane * 4, 0, 16);
          const unsigned id = (unsigned)__builtin_amdgcn_readfirstlane(p) + S_STATIC * 8;
          const int idc = id < (unsigned)NCH ? (int)id : NCH;
          if (lane == 0) STL(&ids[k], idc);
          ++k;
          if (idc >= NCH) break;
        } else {
          __builtin_amdgcn_s_sleep(2);
        }
      }
    }
    TS(5);
    if (LATE_W) tail_weights();
    asm volatile("s_waitcnt lgkmcnt(0)\n s_barrier" ::: "memory");
    TS(6);
    {
      const int j1 = b & 31;
      u32x4 ga[4], gm[4];
      const int vo = (lane >> 3) * 4096 + (lane & 7) * 16, vm = (lane >> 3) * 64;
#pragma unroll 1
      for (int spin = 0; spin < SPIN_MAX; ++spin) {
#pragma unroll
        for (int i = 0; i < 4; ++i) {
          ga[i] = __builtin_amdgcn_raw_buffer_load_b128(rw, vo, WS_H1 + (g * 32 + i * 8) * 4096 + j1 * 128, 16);
          gm[i] = __builtin_amdgcn_raw_buffer_load_b128(rw, vm, WS_ML + (g * 32 + i * 8) * 64 + (j1 >> 4) * 16, 16);
        }
        bool ok = true;
#pragma unroll
        for (int i = 0; i < 4; ++i) ok &= (ga[i][1] == tag) & (ga[i][3] == tag) & (gm[i][1] == tag) & (gm[i][3] == tag);
        if (__builtin_amdgcn_ballot_w64(!ok) == 0) break;
        __builtin_amdgcn_s_sleep(1);
      }
      TS(7);
      float mv[4];
#pragma unroll
      for (int i = 0; i < 4; ++i) { unsigned t0 = gm[i][0]; mv[i] = u2f(t0); }
      float M = __builtin_fmaxf(__builtin_fmaxf(mv[0], mv[1]), __builtin_fmaxf(mv[2], mv[3]));
      M = __builtin_fmaxf(M, dpp<ROR8>(M)); M = sw16_max(M); M = sw32_max(M);
      float L = 0.f, s0 = 0.f, s1 = 0.f;
#pragma unroll
      for (int i = 0; i < 4; ++i) {
        const float f = __builtin_amdgcn_exp2f(mv[i] - M);
        unsigned t0 = ga[i][0], t1 = ga[i][2], t2 = gm[i][2];
        s0 = __builtin_fmaf(f, u2f(t0), s0); s1 = __builtin_fmaf(f, u2f(t1), s1); L = __builtin_fmaf(f, u2f(t2), L);
      }
      s0 += dpp<ROR8>(s0); s1 += dpp<ROR8>(s1); L += dpp<ROR8>(L);
      s0 = sw16_add(s0, s0); s1 = sw16_add(s1, s1); L = sw16_add(L, L);
      s0 = sw32_add(s0, s0); s1 = sw32_add(s1, s1); L = sw32_add(L, L);
      const float rl = __builtin_amdgcn_rcpf(L);
      const unsigned op = pkbf(s0 * rl, s1 * rl);
#pragma unroll
      for (int pc = 0; pc < 8; ++pc) {
        const unsigned os = __builtin_amdgcn_readlane(op, pc);
#pragma unroll
        for (int c = 0; c < 8; ++c) fo[c] = dot2(wp[pc][c], os, fo[c]);
      }
      if (lane < 56) {
#pragma unroll
        for (int i = 0; i < 4; ++i)
          __builtin_amdgcn_raw_buffer_store_b128(u32x4{f2u(fo[2 * i]), tag, f2u(fo[2 * i + 1]), tag}, rw, lane * 16, WS_H2 + b * 4096 + i * 1024, 16);
      }
      TS(8);
    }
  }
}

extern "C" void kernel_launch(void* const* d_in, const int* in_sizes, int n_in, void* d_out, int out_size, void* d_ws,
                              size_t ws_size, hipStream_t stream) {
  fused_decode<<<256, 576, 0, stream>>>((const unsigned*)d_in[0], (const char*)d_in[1], (const char*)d_in[2],
                                        (const char*)d_in[3], (const char*)d_in[4], (const char*)d_in[6],
                                        (const char*)d_in[7], (const char*)d_in[8], (unsigned*)d_out, (char*)d_ws);
}
```

```cpp
#include <hip/hip_runtime.h>
#include <stdint.h>
#include <stdio.h>

typedef unsigned u32x4 __attribute__((ext_vector_type(4)));
typedef unsigned u32x2 __attribute__((ext_vector_type(2)));
typedef float f32x2 __attribute__((ext_vector_type(2)));
typedef float f32x4 __attribute__((ext_vector_type(4)));
typedef __bf16 bf16x2 __attribute__((ext_vector_type(2)));
#define LAS __attribute__((address_space(3)))
typedef __amdgpu_buffer_rsrc_t rsrc_t;

#define S_STATIC 22
#define S_TICK (S_STATIC - 3)
#define NBACK 6
#define ROT 0x100000u
#define THRESH 8
#define GR1 640
#define GR2 256
#define G1 12
#define G2 8
#define G3 4
#define LATE_TICKET 1
#define PW 8
#define TLBW 0
#define HMAP 0
#define HROT 0x200000u
#define PDEL1 0
#define PDEL2 0
#define POLICY 1
#define Q0 4.f
#define FL 8.f
#define GMIN 2
#define RMAX 0
#define XS 8
#define LATEW 2
#define RF 0
#define Q1 4.f
#define STAG 0
#define STAG_A 4
#define STAG_S 4
#define STAG2 0
#define K_AUX 2
#define V_AUX 2

constexpr unsigned REG = 4u << 20;
constexpr unsigned HEADB = 16u << 20;
constexpr int NP = 4 * (256 - 8 * S_STATIC) * 8;
constexpr int NPRE = (S_STATIC - S_TICK) * 8;
static_assert(NPRE <= 64 && S_TICK >= 2 * NBACK + 1, "");
constexpr int UCAP = 448;
static_assert(RMAX + NP / (UCAP - NPRE - 2 * G1) <= 31, "someone stays until the pool is empty");
constexpr unsigned OOB = 0x40000000u, EMPTY = 0xffffffffu;
constexpr int SPIN_MAX = 200000;
constexpr int NC2 = 224 / PW;
constexpr unsigned WS_EPOCH = 0, WS_CTR = 4096, WS_Q = 16384, WS_ML = 32768, WS_H1 = 65536, WS_H2 = WS_H1 + (1u << 20);
constexpr unsigned WS_F = 2u << 20, WS_USED = WS_F + 224 * 1024;
constexpr float CSC = 0.0625f * 1.4426950408889634f;

template <int V> struct IC { static constexpr int v = V; };
__device__ __forceinline__ float u2f(unsigned u) { return __builtin_bit_cast(float, u); }
__device__ __forceinline__ unsigned f2u(float f) { return __builtin_bit_cast(unsigned, f); }
__device__ __forceinline__ float rbf(float f) { __bf16 b = (__bf16)f; return (float)b; }
__device__ __forceinline__ unsigned pkbf(float lo, float hi) {
  bf16x2 v; v[0] = (__bf16)lo; v[1] = (__bf16)hi; return __builtin_bit_cast(unsigned, v);
}
__device__ __forceinline__ float dot2(unsigned a, unsigned b, float acc) {
  return __builtin_amdgcn_fdot2_f32_bf16(__builtin_bit_cast(bf16x2, a), __builtin_bit_cast(bf16x2, b), acc, false);
}
template <int CTRL> __device__ __forceinline__ float dpp(float x) {
  return __builtin_bit_cast(float, __builtin_amdgcn_mov_dpp(__builtin_bit_cast(int, x), CTRL, 0xf, 0xf, true));
}
constexpr int XOR1 = 0xB1, XOR2 = 0x4E, XOR7 = 0x141, ROR8 = 0x128, ROR4 = 0x124, BC0 = 0x150, BC8 = 0x158;
__device__ __forceinline__ float sw16_add(float a, float b) {
  auto s = __builtin_amdgcn_permlane16_swap(f2u(a), f2u(b), false, false);
  unsigned s0 = s[0], s1 = s[1]; return u2f(s0) + u2f(s1);
}
__device__ __forceinline__ float sw32_add(float a, float b) {
  auto s = __builtin_amdgcn_permlane32_swap(f2u(a), f2u(b), false, false);
  unsigned s0 = s[0], s1 = s[1]; return u2f(s0) + u2f(s1);
}
__device__ __forceinline__ float sw16_max(float a) {
  auto s = __builtin_amdgcn_permlane16_swap(f2u(a), f2u(a), false, false);
  unsigned s0 = s[0], s1 = s[1]; return __builtin_fmaxf(u2f(s0), u2f(s1));
}
__device__ __forceinline__ float sw32_max(float a) {
  auto s = __builtin_amdgcn_permlane32_swap(f2u(a), f2u(a), false, false);
  unsigned s0 = s[0], s1 = s[1]; return __builtin_fmaxf(u2f(s0), u2f(s1));
}
__device__ __forceinline__ float wave_sum(float t) {
  t += dpp<XOR1>(t); t += dpp<XOR2>(t); t += dpp<XOR7>(t); t += dpp<ROR8>(t);
  t = sw16_add(t, t); return sw32_add(t, t);
}
__device__ __forceinline__ float rdl(float v, int l) { return u2f(__builtin_amdgcn_readlane(f2u(v), l)); }
#define LDL(p) __hip_atomic_load((LAS int*)(p), __ATOMIC_RELAXED, __HIP_MEMORY_SCOPE_WORKGROUP)
#define STL(p, v) __hip_atomic_store((LAS int*)(p), v, __ATOMIC_RELAXED, __HIP_MEMORY_SCOPE_WORKGROUP)
#define SCHED() __builtin_amdgcn_sched_barrier(0)

#define TS(i) do {} while (0)

template <bool REFILL>
__device__ __forceinline__ void step(u32x4 (&kr)[2], u32x4 (&vr)[2], const u32x4& q0, const u32x4& q1, f32x2 (&a0)[4],
                                     f32x2 (&a1)[4], float& m, float& mc, float& l, rsrc_t rk, rsrc_t rv, int voff,
                                     unsigned o, bool lane8) {
  float d[4];
#pragma unroll
  for (int u = 0; u < 2; ++u) {
    const u32x4 k = kr[u];
    float x = 0.f, y = 0.f;
#pragma unroll
    for (int e = 0; e < 4; ++e) { unsigned w = k[e], qa = q0[e], qb = q1[e]; x = dot2(w, qa, x); y = dot2(w, qb, y); }
    if (REFILL) {
      asm volatile("" : "+v"(x), "+v"(y) :: "memory");
      kr[u] = __builtin_amdgcn_raw_buffer_load_b128(rk, voff + u * 1024, o, K_AUX);
      SCHED();
    }
    d[2 * u] = x; d[2 * u + 1] = y;
  }
  float ab = sw16_add(d[0], d[1]), cd = sw16_add(d[2], d[3]);
  ab += dpp<ROR8>(ab); cd += dpp<ROR8>(cd);
  float t = lane8 ? cd : ab;
  t += dpp<XOR7>(t); t += dpp<XOR2>(t); t += dpp<XOR1>(t);
  t = rbf(t);
  if (__builtin_amdgcn_ballot_w64(t > m) != 0) {
    float mx = __builtin_fmaxf(t, dpp<ROR8>(t));
    mx = __builtin_fmaxf(sw32_max(mx), m);
    const float mcn = mx * CSC;
    const float f = __builtin_amdgcn_exp2f(mc - mcn);
    m = mx; mc = mcn; l *= f;
    const float f0 = rdl(f, 0), f1 = rdl(f, 16);
#pragma unroll
    for (int k = 0; k < 4; ++k) { a0[k] *= f0; a1[k] *= f1; }
  }
  const float p = __builtin_amdgcn_exp2f(__builtin_fmaf(t, CSC, -mc));
  l += p;
  auto s = __builtin_amdgcn_permlane16_swap(f2u(p), f2u(p), false, false);
  unsigned s0 = s[0], s1 = s[1];
  const float ph0 = u2f(s0), ph1 = u2f(s1);
  const float pp[4] = {dpp<BC0>(ph0), dpp<BC0>(ph1), dpp<BC8>(ph0), dpp<BC8>(ph1)};
#pragma unroll
  for (int u = 0; u < 2; ++u) {
    const u32x4 v = vr[u];
    const f32x2 p0 = {pp[2 * u], pp[2 * u]}, p1 = {pp[2 * u + 1], pp[2 * u + 1]};
#pragma unroll
    for (int e = 0; e < 4; ++e) {
      unsigned w = v[e];
      const f32x2 x = {u2f(w << 16), u2f(w & 0xffff0000u)};
      a0[e] = __builtin_elementwise_fma(x, p0, a0[e]);
      a1[e] = __builtin_elementwise_fma(x, p1, a1[e]);
    }
    if (REFILL) {
      asm volatile("" : "+v"(a0[0]), "+v"(a0[1]), "+v"(a0[2]), "+v"(a0[3]), "+v"(a1[0]), "+v"(a1[1]), "+v"(a1[2]), "+v"(a1[3]) :: "memory");
      vr[u] = __builtin_amdgcn_raw_buffer_load_b128(rv, voff + u * 1024, o, V_AUX);
      SCHED();
    }
  }
}

__device__ __forceinline__ void gemv16(const u32x4 (&w)[14], const unsigned (&xp)[7], float& r0, float& r1) {
  float a[8];
#pragma unroll
  for (int i = 0; i < 8; ++i) a[i] = 0.f;
#pragma unroll
  for (int j = 0; j < 7; ++j) {
#pragma unroll
    for (int k = 0; k < 4; ++k) {
      unsigned w0 = w[2 * j][k], w1 = w[2 * j + 1][k];
      a[2 * k] = dot2(__builtin_amdgcn_perm(w1, w0, 0x05040100u), xp[j], a[2 * k]);
      a[2 * k + 1] = dot2(__builtin_amdgcn_perm(w1, w0, 0x07060302u), xp[j], a[2 * k + 1]);
    }
  }
  float b[4];
#pragma unroll
  for (int i = 0; i < 4; ++i) b[i] = sw32_add(a[i], a[i + 4]);
  r0 = sw16_add(b[0], b[2]); r1 = sw16_add(b[1], b[3]);
  r0 += dpp<ROR8>(r0); r1 += dpp<ROR8>(r1);
  r0 += dpp<ROR4>(r0); r1 += dpp<ROR4>(r1);
  r0 += dpp<XOR2>(r0); r1 += dpp<XOR2>(r1);
}

__device__ __forceinline__ void put(rsrc_t rw, unsigned off, unsigned a, unsigned b, unsigned tag) {
  __builtin_amdgcn_raw_buffer_store_b128(u32x4{a, tag, b, tag}, rw, off, 0, 16);
}

template <int N>
__device__ __forceinline__ void gather(rsrc_t rw, unsigned (&off)[N], unsigned tag, float (&v0)[N], float (&v1)[N]) {
#pragma unroll
  for (int i = 0; i < N; ++i) { v0[i] = 0.f; v1[i] = 0.f; }
#pragma unroll 1
  for (int spin = 0; spin < SPIN_MAX; ++spin) {
    u32x4 t[N];
#pragma unroll
    for (int i = 0; i < N; ++i) t[i] = __builtin_amdgcn_raw_buffer_load_b128(rw, off[i], 0, 16);
    bool left = false;
#pragma unroll
    for (int i = 0; i < N; ++i) {
      const unsigned t0 = t[i][0], t1 = t[i][1], t2 = t[i][2], t3 = t[i][3];
      const bool ok = (t1 == tag) & (t3 == tag) & (off[i] != OOB);
      v0[i] = ok ? u2f(t0) : v0[i]; v1[i] = ok ? u2f(t2) : v1[i];
      off[i] = ok ? OOB : off[i];
      left |= off[i] != OOB;
    }
    if (__builtin_amdgcn_ballot_w64(left) == 0) break;
    __builtin_amdgcn_s_sleep(1);
  }
}

template <int N> __device__ __forceinline__ void poll_issue(rsrc_t rw, const unsigned (&off)[N], u32x4 (&t)[N]) {
#pragma unroll
  for (int i = 0; i < N; ++i) t[i] = __builtin_amdgcn_raw_buffer_load_b128(rw, off[i], 0, 16);
  SCHED();
}
template <int N>
__device__ __forceinline__ bool poll_check(const u32x4 (&t)[N], unsigned (&off)[N], unsigned tag, float (&v0)[N], float (&v1)[N]) {
  bool left = false;
#pragma unroll
  for (int i = 0; i < N; ++i) {
    const unsigned t0 = t[i][0], t1 = t[i][1], t2 = t[i][2], t3 = t[i][3];
    const bool ok = (t1 == tag) & (t3 == tag) & (off[i] != OOB);
    v0[i] = ok ? u2f(t0) : v0[i]; v1[i] = ok ? u2f(t2) : v1[i];
    off[i] = ok ? OOB : off[i];
    left |= off[i] != OOB;
  }
  return __builtin_amdgcn_ballot_w64(left) == 0;
}
template <int N>
__device__ __forceinline__ void poll2(rsrc_t rw, unsigned (&off)[N], unsigned tag, float (&v0)[N], float (&v1)[N],
                                      u32x4 (&ta)[N], u32x4 (&tb)[N]) {
#pragma unroll 1
  for (int spin = 0; spin < SPIN_MAX; ++spin) {
    if (poll_check<N>(ta, off, tag, v0, v1)) break;
    poll_issue<N>(rw, off, ta);
    if (poll_check<N>(tb, off, tag, v0, v1)) break;
    poll_issue<N>(rw, off, tb);
  }
}

__global__ void __launch_bounds__(576) fused_decode(const unsigned* __restrict__ x32, const char* __restrict__ K,
                                                    const char* __restrict__ V, const char* __restrict__ wq,
                                                    const char* __restrict__ wo, const char* __restrict__ wg,
                                                    const char* __restrict__ wu, const char* __restrict__ wd,
                                                    unsigned* out32, char* ws) {
  __shared__ __attribute__((aligned(16))) float macc[8 * 512];
  __shared__ __attribute__((aligned(16))) unsigned qs[256];
  __shared__ __attribute__((aligned(16))) unsigned xs[224];
  __shared__ __attribute__((aligned(16))) float mml[32];
  __shared__ __attribute__((aligned(16))) float red[8 * 16];
  __shared__ __attribute__((aligned(16))) unsigned uaddr[512];
  __shared__ int ctl[8];
  const int lane = threadIdx.x & 63, hwave = __builtin_amdgcn_readfirstlane(threadIdx.x >> 6);
  const int wave = hwave == 0 ? 8 : hwave - 1;
  const int b = blockIdx.x, g = b >> 5, grp = b >> 3, mem = b & 7, j32 = b & 31;
  asm volatile("" :: "s"(x32), "s"(K), "s"(V), "s"(wq), "s"(wo), "s"(wg), "s"(wu), "s"(wd), "s"(out32), "s"(ws));
  TS(0);
  const rsrc_t rw = __builtin_amdgcn_make_buffer_rsrc((void*)ws, 0, WS_USED, 0x00020000);
  const rsrc_t rk = __builtin_amdgcn_make_buffer_rsrc((void*)(K + (size_t)g * HEADB), 0, HEADB, 0x00020000);
  const rsrc_t rv = __builtin_amdgcn_make_buffer_rsrc((void*)(V + (size_t)g * HEADB), 0, HEADB, 0x00020000);
  unsigned tag;

  if (__builtin_expect(wave < 8, 1)) {
    const int voff = lane * 16;
    const unsigned sbase = (grp & 3) * REG + wave * 2048, srot = grp * ROT + mem * 16384u;
    auto offs = [&](int s) -> unsigned {
      return HMAP ? ((s * 524288u + j32 * 16384u + g * HROT) & (HEADB - 1)) + wave * 2048 : sbase + ((s * 131072u + srot) & (REG - 1));
    };
    u32x4 kb[NBACK + 1][2], vb[NBACK + 1][2];
    asm volatile("s_barrier" ::: "memory"); SCHED();
#pragma unroll
    for (int s = 0; s <= NBACK; ++s) {
      const unsigned o = offs(s);
#pragma unroll
      for (int u = 0; u < 2; ++u) {
        if (s > 0) asm volatile("s_waitcnt vmcnt(3)" ::: "memory");
        kb[s][u] = __builtin_amdgcn_raw_buffer_load_b128(rk, voff + u * 1024, o, K_AUX);
        SCHED();
      }
#pragma unroll
      for (int u = 0; u < 2; ++u) {
        if (s > 0) asm volatile("s_waitcnt vmcnt(3)" ::: "memory");
        vb[s][u] = __builtin_amdgcn_raw_buffer_load_b128(rv, voff + u * 1024, o, V_AUX);
        SCHED();
      }
    }
    TS(1);
    {
      int spin = 0;
#pragma unroll 1
      while (__builtin_amdgcn_readfirstlane(LDL(&ctl[1])) == 0 && ++spin < SPIN_MAX) __builtin_amdgcn_s_sleep(1);
    }
    asm volatile("" ::: "memory");
    TS(2);
    const u32x4 q0 = *(const u32x4*)&qs[(lane & 31) * 4], q1 = *(const u32x4*)&qs[128 + (lane & 31) * 4];
    tag = __builtin_amdgcn_readfirstlane(LDL(&ctl[2]));
    f32x2 a0[4], a1[4];
#pragma unroll
    for (int k = 0; k < 4; ++k) { a0[k] = f32x2{0.f, 0.f}; a1[k] = f32x2{0.f, 0.f}; }
    float m = -INFINITY, mc = -INFINITY, l = 0.f;
    const bool lane8 = (lane & 8) != 0;
#pragma unroll
    for (int s = 0; s < NBACK; ++s) {
      step<true>(kb[NBACK], vb[NBACK], q0, q1, a0, a1, m, mc, l, rk, rv, voff, offs(NBACK + 1 + s), lane8);
      step<false>(kb[s], vb[s], q0, q1, a0, a1, m, mc, l, rk, rv, voff, 0, lane8);
      SCHED();
    }
    TS(3);
    int n = 2 * NBACK + 1;
#pragma unroll 1
    for (;;) {
      unsigned o;
      if (n < S_TICK) {
        o = offs(n);
      } else {
        if (LATE_TICKET) asm volatile("s_waitcnt vmcnt(3)" ::: "memory");
        int t = 0;
        if (lane == 0) t = __hip_atomic_fetch_add((LAS int*)&ctl[0], 1, __ATOMIC_RELAXED, __HIP_MEMORY_SCOPE_WORKGROUP);
        t = __builtin_amdgcn_readfirstlane(t);
        o = __builtin_amdgcn_readfirstlane(LDL(&uaddr[t]));
        int spin = 0;
#pragma unroll 1
        while (o == EMPTY && ++spin < SPIN_MAX) { __builtin_amdgcn_s_sleep(1); o = __builtin_amdgcn_readfirstlane(LDL(&uaddr[t])); }
        o = o == EMPTY ? OOB : o;
      }
      step<true>(kb[NBACK], vb[NBACK], q0, q1, a0, a1, m, mc, l, rk, rv, voff, o, lane8);
      if (o == OOB) break;
      ++n;
    }
    TS(4);
    f32x4 lo, hi;
    lo[0] = sw32_add(a0[0][0], a1[0][0]); lo[1] = sw32_add(a0[0][1], a1[0][1]);
    lo[2] = sw32_add(a0[1][0], a1[1][0]); lo[3] = sw32_add(a0[1][1], a1[1][1]);
    hi[0] = sw32_add(a0[2][0], a1[2][0]); hi[1] = sw32_add(a0[2][1], a1[2][1]);
    hi[2] = sw32_add(a0[3][0], a1[3][0]); hi[3] = sw32_add(a0[3][1], a1[3][1]);
    *(f32x4*)&macc[wave * 512 + lane * 4] = lo;
    *(f32x4*)&macc[wave * 512 + 256 + lane * 4] = hi;
    l += dpp<ROR8>(l); l = sw32_add(l, l);
    if ((lane & 47) == 0) *(f32x2*)&mml[(lane >> 4) * 16 + wave * 2] = f32x2{mc, l};
    asm volatile("s_waitcnt lgkmcnt(0)" ::: "memory");
    int arr = 0;
    if (lane == 0) arr = __hip_atomic_fetch_add((LAS int*)&ctl[3], 1, __ATOMIC_RELAXED, __HIP_MEMORY_SCOPE_WORKGROUP);
    arr = __builtin_amdgcn_readfirstlane(arr);
    asm volatile("" ::: "memory");
    if (arr == 7) {
      TS(5);
      f32x4 ml[4], pl[8], ph[8];
#pragma unroll
      for (int i = 0; i < 4; ++i) ml[i] = *(const f32x4*)&mml[(lane >> 5) * 16 + i * 4];
#pragma unroll
      for (int w = 0; w < 8; ++w) { pl[w] = *(const f32x4*)&macc[w * 512 + lane * 4]; ph[w] = *(const f32x4*)&macc[w * 512 + 256 + lane * 4]; }
      float M = ml[0][0];
#pragma unroll
      for (int i = 0; i < 4; ++i) M = __builtin_fmaxf(M, __builtin_fmaxf(ml[i][0], ml[i][2]));
      float L = 0.f; f32x4 sl = {0.f, 0.f, 0.f, 0.f}, sh = {0.f, 0.f, 0.f, 0.f};
#pragma unroll
      for (int w = 0; w < 8; ++w) {
        const float f = __builtin_amdgcn_exp2f(ml[w >> 1][(w & 1) * 2] - M);
        L = __builtin_fmaf(f, ml[w >> 1][(w & 1) * 2 + 1], L);
        sl = __builtin_elementwise_fma(pl[w], f32x4{f, f, f, f}, sl);
        sh = __builtin_elementwise_fma(ph[w], f32x4{f, f, f, f}, sh);
      }
      const float sv[8] = {sl[0], sl[1], sl[2], sl[3], sh[0], sh[1], sh[2], sh[3]};
      const int vo = (lane >> 3) * 16384 + (lane & 7) * 16;
#pragma unroll
      for (int i = 0; i < 4; ++i)
        put(rw, WS_H1 + g * 131072 + i * 4096 + j32 * 128 + (vo), f2u(sv[2 * i]), f2u(sv[2 * i + 1]), tag);
      if ((lane & 31) == 0)
        put(rw, WS_ML + g * 1024 + j32 * 16 + ((lane >> 5) * 512), f2u(M), f2u(L), tag);
      TS(6);
    }
    const int c2 = PW == 8 ? b / 9 : (b & 1) * 28 + (b >> 1) / 9 * 2 + (((b >> 1) % 9) >> 2);
    const bool cons = PW == 8 ? (b % 9 == 0 && c2 < 28) : false;
    if (cons) {
      const int ow = (c2 >> 2) * (PW * 4) + (lane & (PW - 1)) * 4 + (c2 & 3);
      const unsigned xv = x32[ow];
      unsigned off[PW / 2]; float v0[PW / 2], v1[PW / 2];
#pragma unroll
      for (int i = 0; i < PW / 2; ++i) off[i] = WS_H2 + (c2 * 256 + wave * 32) * (PW * 16) + i * 1024 + lane * 16;
      if (PDEL2) {
        int spin = 0;
#pragma unroll 1
        while (__builtin_amdgcn_readfirstlane(LDL(&ctl[5])) == 0 && ++spin < SPIN_MAX) __builtin_amdgcn_s_sleep(1);
        asm volatile("" ::: "memory");
      }
      u32x4 ta[PW / 2], tb[PW / 2];
      if (STAG2) {
#pragma unroll
        for (int i = 0; i < PW / 2; ++i) { v0[i] = 0.f; v1[i] = 0.f; }
        poll_issue<PW / 2>(rw, off, ta);
        __builtin_amdgcn_s_sleep(STAG2);
        poll_issue<PW / 2>(rw, off, tb);
        poll2<PW / 2>(rw, off, tag, v0, v1, ta, tb);
      } else gather<PW / 2>(rw, off, tag, v0, v1);
      TS(7);
      float s0 = 0.f, s1 = 0.f;
#pragma unroll
      for (int i = 0; i < PW / 2; ++i) { s0 += v0[i]; s1 += v1[i]; }
      float s = sw32_add(s0, s1);
      s += dpp<ROR8>(s); s = sw16_add(s, s);
      if (PW == 4) s += dpp<ROR4>(s);
      if ((lane & 24) == 0) red[wave * 16 + (lane & 7) * 2 + (lane >> 5)] = s;
      asm volatile("s_waitcnt lgkmcnt(0)" ::: "memory");
      int fin = 0;
      if (lane == 0) fin = __hip_atomic_fetch_add((LAS int*)&ctl[4], 1, __ATOMIC_RELAXED, __HIP_MEMORY_SCOPE_WORKGROUP);
      fin = __builtin_amdgcn_readfirstlane(fin);
      asm volatile("" ::: "memory");
      if (fin == 7) {
        const f32x2 r = *(const f32x2*)&red[lane * 2];
        float r0 = r[0], r1 = r[1];
        r0 += dpp<ROR8>(r0); r1 += dpp<ROR8>(r1);
        r0 = sw16_add(r0, r0); r1 = sw16_add(r1, r1);
        r0 = sw32_add(r0, r0); r1 = sw32_add(r1, r1);
        if (lane < PW) out32[ow] = pkbf(r0 + u2f(xv << 16), r1 + u2f(xv & 0xffff0000u));
        if (b == 0 && lane == 0) __builtin_amdgcn_raw_buffer_store_b32(tag, rw, 0, WS_EPOCH, 16);
        TS(8);
      }
      if (STAG2) asm volatile("" :: "v"(ta[0]), "v"(ta[1]), "v"(ta[2]), "v"(ta[3]), "v"(tb[0]), "v"(tb[1]), "v"(tb[2]), "v"(tb[3]));
    }
    asm volatile("" :: "v"(kb[NBACK][0]), "v"(kb[NBACK][1]), "v"(vb[NBACK][0]), "v"(vb[NBACK][1]));
  } else {
    __builtin_amdgcn_s_setprio(3);
    const unsigned ep = __builtin_amdgcn_raw_buffer_load_b32(rw, 0, WS_EPOCH, 16);
    unsigned xr[7];
#pragma unroll
    for (int j = 0; j < 7; ++j) xr[j] = x32[(lane >> 1) + 32 * j];
    u32x4 w[14];
    const int jq = mem * 4 + (grp & 3);
    {
      const rsrc_t r = __builtin_amdgcn_make_buffer_rsrc((void*)wq, 0, 448 * 8192, 0x00020000);
      const int vo = (lane >> 1) * 16384 + (lane & 1) * 16;
#pragma unroll
      for (int i = 0; i < 14; ++i) w[i] = __builtin_amdgcn_raw_buffer_load_b128(r, vo, g * 1024 + jq * 32 + (i >> 1) * 524288 + (i & 1) * 8192, 2);
    }
    if (j32 == 0 && lane == 0) __builtin_amdgcn_raw_buffer_store_b32(0u, rw, 0, WS_CTR + g * 256, 16);
    SCHED();
    *(u32x4*)&uaddr[lane * 4] = u32x4{EMPTY, EMPTY, EMPTY, EMPTY};
    *(u32x4*)&uaddr[256 + lane * 4] = u32x4{EMPTY, EMPTY, EMPTY, EMPTY};
    if (HMAP) { if (lane < NPRE) uaddr[lane] = (((S_TICK + (lane >> 3)) * 524288u + j32 * 16384u + g * HROT) & (HEADB - 1)) + (lane & 7) * 2048; }
    else if (lane < NPRE) uaddr[lane] = (grp & 3) * REG + (lane & 7) * 2048 + (((S_TICK + (lane >> 3)) * 131072u + grp * ROT + mem * 16384u) & (REG - 1));
    if (lane < 8) ctl[lane] = 0;
    asm volatile("s_waitcnt lgkmcnt(0)\n s_barrier" ::: "memory");
    asm volatile("" : "+v"(xr[0]), "+v"(xr[1]), "+v"(xr[2]), "+v"(xr[3]), "+v"(xr[4]), "+v"(xr[5]), "+v"(xr[6]));
    unsigned xp[7];
    {
      float s = 0.f;
#pragma unroll
      for (int j = 0; j < 7; ++j) s += u2f(xr[j] << 16) + u2f(xr[j] & 0xffff0000u);
      const float mean = wave_sum(s) * (1.f / 896.f);
      float v = 0.f;
#pragma unroll
      for (int j = 0; j < 7; ++j) { const float da = u2f(xr[j] << 16) - mean, db = u2f(xr[j] & 0xffff0000u) - mean; v += da * da + db * db; }
      const float var = wave_sum(v) * (1.f / 896.f);
      const float mb = rbf(mean), rden = __builtin_amdgcn_rcpf(rbf(__builtin_amdgcn_sqrtf(rbf(rbf(var) + 1e-5f))));
#pragma unroll
      for (int j = 0; j < 7; ++j) xp[j] = pkbf(rbf(u2f(xr[j] << 16) - mb) * rden, rbf(u2f(xr[j] & 0xffff0000u) - mb) * rden);
    }
    tag = __builtin_amdgcn_readfirstlane(ep) + 1u;
    TS(1);
    {
      float r0, r1;
      gemv16(w, xp, r0, r1);
      const unsigned pk = pkbf(r0, r1);
      auto s = __builtin_amdgcn_permlane16_swap(pk, pk, false, false);
      unsigned s0 = s[0], s1 = s[1];
      asm volatile("s_waitcnt vmcnt(0)" : "+v"(s0), "+v"(s1) :: "memory");
      if ((lane & 30) == 0)
        put(rw, WS_Q + g * 2048 + jq * 64 + (((lane & 1) * 2 + (lane >> 5)) * 16), s0, s1, tag);
    }
    TS(2);
#pragma unroll
    for (int j = 0; j < 7; ++j) xs[(lane >> 1) + 32 * j] = xp[j];
    {
      unsigned off[2] = {WS_Q + g * 2048 + lane * 16u, WS_Q + g * 2048 + 1024 + lane * 16u};
      float v0[2], v1[2];
      gather<2>(rw, off, tag, v0, v1);
      *(f32x2*)&qs[lane * 2] = f32x2{v0[0], v1[0]};
      *(f32x2*)&qs[128 + lane * 2] = f32x2{v0[1], v1[1]};
      if (lane == 0) STL(&ctl[2], (int)tag);
      asm volatile("s_waitcnt lgkmcnt(0)" ::: "memory");
      if (lane == 0) STL(&ctl[1], 1);
    }
    TS(3);
    const int fu = grp * 7 + mem;
    if (mem < 7) {
      const rsrc_t rg = __builtin_amdgcn_make_buffer_rsrc((void*)wg, 0, 448 * 3584, 0x00020000);
      const rsrc_t ru = __builtin_amdgcn_make_buffer_rsrc((void*)wu, 0, 448 * 3584, 0x00020000);
      const int vo = (lane >> 3) * 3584 + (lane & 7) * 16, so = (fu & 7) * 56 * 3584 + (fu >> 3) * 128;
#pragma unroll
      for (int i = 0; i < 7; ++i) {
        w[2 * i] = __builtin_amdgcn_raw_buffer_load_b128(rg, vo, so + i * 8 * 3584, 2);
        w[2 * i + 1] = __builtin_amdgcn_raw_buffer_load_b128(ru, vo, so + i * 8 * 3584, 2);
      }
      float a[16];
#pragma unroll
      for (int i = 0; i < 16; ++i) a[i] = 0.f;
#pragma unroll
      for (int i = 0; i < 7; ++i) {
        const unsigned xl = ((const unsigned short*)xs)[(fu & 7) * 56 + i * 8 + (lane >> 3)], xh = xl << 16;
#pragma unroll
        for (int e = 0; e < 4; ++e) {
          const unsigned wa = w[2 * i][e], wb = w[2 * i + 1][e];
          a[2 * e] = dot2(wa, xl, a[2 * e]); a[2 * e + 1] = dot2(wa, xh, a[2 * e + 1]);
          a[8 + 2 * e] = dot2(wb, xl, a[8 + 2 * e]); a[8 + 2 * e + 1] = dot2(wb, xh, a[8 + 2 * e + 1]);
        }
      }
#pragma unroll
      for (int i = 0; i < 8; ++i) a[i] = sw32_add(a[i], a[i + 8]);
#pragma unroll
      for (int i = 0; i < 4; ++i) { a[i] = sw16_add(a[i], a[i + 4]); a[i] += dpp<ROR8>(a[i]); }
      const bool b3 = (lane & 8) != 0;
      const int src = (((lane & 7) << 3) | (lane >> 3)) * 4;
      const unsigned d0 = __builtin_amdgcn_ds_bpermute(src, f2u(b3 ? a[2] : a[0])), d1 = __builtin_amdgcn_ds_bpermute(src, f2u(b3 ? a[3] : a[1]));
      put(rw, WS_F + (fu >> 3) * 8192 + (fu & 7) * 128 + ((lane >> 3) * 1024 + (lane & 7) * 16), d0, d1, tag);
    }
    SCHED();
    float fo[8];
    unsigned wp[8][8], hp = 0;
    const rsrc_t ro = __builtin_amdgcn_make_buffer_rsrc((void*)wo, 0, 4096 * 896, 0x00020000);
    const rsrc_t rd = __builtin_amdgcn_make_buffer_rsrc((void*)wd, 0, 1792 * 896, 0x00020000);
    const int row0 = g * 512 + (j32 >> 2) * 64 + (j32 & 3) * 2;
    const unsigned wvo = lane < 56 ? lane * 16u : OOB;
    u32x4 ov[16], dv[8];
    auto issue_w = [&]() __attribute__((always_inline)) {
#pragma unroll
      for (int i = 0; i < 8; ++i) dv[i] = __builtin_amdgcn_raw_buffer_load_b128(rd, wvo, mem < 7 ? (fu * 8 + i) * 896 : OOB, 2);
#pragma unroll
      for (int i = 0; i < 16; ++i) ov[i] = __builtin_amdgcn_raw_buffer_load_b128(ro, wvo, (row0 + (i >> 1) * 8 + (i & 1)) * 896, 2);
      SCHED();
    };
    auto prep_d = [&]() __attribute__((always_inline)) {
#pragma unroll
      for (int i = 0; i < 8; ++i) fo[i] = 0.f;
#pragma unroll
      for (int pc = 0; pc < 4; ++pc) {
        const unsigned hs = __builtin_amdgcn_readlane(hp, pc);
#pragma unroll
        for (int k = 0; k < 4; ++k) {
          unsigned w0 = dv[2 * pc][k], w1 = dv[2 * pc + 1][k];
          fo[2 * k] = dot2(__builtin_amdgcn_perm(w1, w0, 0x05040100u), hs, fo[2 * k]);
          fo[2 * k + 1] = dot2(__builtin_amdgcn_perm(w1, w0, 0x07060302u), hs, fo[2 * k + 1]);
        }
      }
      asm volatile("" : "+v"(fo[0]), "+v"(fo[1]), "+v"(fo[2]), "+v"(fo[3]), "+v"(fo[4]), "+v"(fo[5]), "+v"(fo[6]), "+v"(fo[7]));
      SCHED();
    };
    auto prep_o = [&](auto p0, auto p1) __attribute__((always_inline)) {
#pragma unroll
      for (int pc = decltype(p0)::v; pc < decltype(p1)::v; ++pc)
#pragma unroll
        for (int k = 0; k < 4; ++k) {
          unsigned w0 = ov[2 * pc][k], w1 = ov[2 * pc + 1][k];
          wp[pc][2 * k] = __builtin_amdgcn_perm(w1, w0, 0x05040100u);
          wp[pc][2 * k + 1] = __builtin_amdgcn_perm(w1, w0, 0x07060302u);
          asm volatile("" : "+v"(wp[pc][2 * k]), "+v"(wp[pc][2 * k + 1]));
        }
      SCHED();
    };
    if (!LATEW) issue_w();
    else {
      const unsigned t0 = __builtin_amdgcn_raw_buffer_load_b32(ro, 0, row0 * 896, 2), t1 = __builtin_amdgcn_raw_buffer_load_b32(rd, 0, fu * 8 * 896, 2);
      const unsigned t2 = __builtin_amdgcn_raw_buffer_load_b32(rw, WS_H1 + b * 4096 + lane * 64, 0, 16);
      const unsigned t3 = __builtin_amdgcn_raw_buffer_load_b32(rw, WS_ML + g * 1024 + lane * 16, 0, 16);
      asm volatile("" :: "v"(t0), "v"(t1), "v"(t2), "v"(t3));
    }
    if (mem < 7) {
      unsigned off[1] = {WS_F + fu * 1024 + lane * 16u};
      float v0[1], v1[1];
      gather<1>(rw, off, tag, v0, v1);
      float r0 = v0[0], r1 = v1[0];
      r0 += dpp<ROR8>(r0); r1 += dpp<ROR8>(r1);
      r0 = sw16_add(r0, r0); r1 = sw16_add(r1, r1);
      r0 = rbf(sw32_add(r0, r0)); r1 = rbf(sw32_add(r1, r1));
      const float u0 = dpp<ROR4>(r0), u1 = dpp<ROR4>(r1);
      const float s0 = rbf(r0 * __builtin_amdgcn_rcpf(1.f + __expf(-r0))), s1 = rbf(r1 * __builtin_amdgcn_rcpf(1.f + __expf(-r1)));
      hp = pkbf(s0 * u0, s1 * u1);
    }
    if (!LATEW) { prep_d(); prep_o(IC<0>{}, IC<8>{}); }
    TS(4);
    {
      const rsrc_t rc = __builtin_amdgcn_make_buffer_rsrc((void*)(ws + WS_CTR + g * 256), 0, 4, 0x00020000);
      int k = NPRE, want = G1 + 1, kfin = -1;
      bool asked = false;
#pragma unroll 1
      for (int spin = 0; spin < 16 * SPIN_MAX; ++spin) {
        const int t = __builtin_amdgcn_readfirstlane(LDL(&ctl[0]));
        if (k - t <= THRESH) {
          const bool fin = want <= G1 && !asked;
          const int gr = want > G1 ? G1 : !fin && k - t <= 0 && want < GMIN ? GMIN : want;
          const unsigned r = (unsigned)__builtin_amdgcn_readfirstlane(__builtin_amdgcn_raw_ptr_buffer_atomic_add_i32(gr + (fin ? 1 << 24 : 0), rc, lane * 4, 0, 16));
          const unsigned p = r & 0xffffffu;
          const int rem = NP - (int)p - gr, nr = (int)(r >> 24);
          const bool last = rem <= 0 || k + gr + G1 > UCAP || (fin && nr < RMAX && rem <= (31 - nr) * XS);
          asked |= fin;
          const unsigned pu = p + lane, jc = pu >> 3, rr = jc & 3;
          const unsigned o = HMAP ? (S_STATIC * 524288u + pu * 2048u + g * HROT) & (HEADB - 1) : rr * REG + (pu & 7) * 2048 + (((S_STATIC * 8 + (jc >> 2)) * 16384u + (g * 4 + rr) * ROT) & (REG - 1));
          const bool valid = pu < (unsigned)NP && lane < gr;
          if (valid || last) STL(&uaddr[k + lane], (int)(valid ? o : OOB));
          if (last) break;
          k += gr;
          if (rem >= RF || kfin < 0 || asked) {
            const int t2 = __builtin_amdgcn_readfirstlane(LDL(&ctl[0]));
            const float ta = 0.5f * (float)(t + t2), q = rem >= RF ? Q0 : Q1;
            const float rho = (S_TICK * 8 + ta) * __builtin_amdgcn_rcpf(256.f - q - rem * (1.f / 32.f));
            kfin = __builtin_amdgcn_readfirstlane((int)(rho * (rem * (1.f / 32.f) + q + FL) - FL + ta + 0.5f));
          }
          want = kfin - k < 0 ? 0 : kfin - k;
        } else {
          __builtin_amdgcn_s_sleep(1);
        }
      }
    }
    TS(5);
    {
      unsigned off[5]; float v0[5], v1[5];
      u32x4 ta[5], tb[5];
#pragma unroll
      for (int i = 0; i < 4; ++i) off[i] = WS_H1 + b * 4096 + i * 1024 + lane * 16;
      off[4] = WS_ML + g * 1024 + (j32 >> 4) * 512 + ((lane >> 3) + 8 * (lane & 3)) * 16;
#pragma unroll
      for (int i = 0; i < 5; ++i) { v0[i] = 0.f; v1[i] = 0.f; }
      if (LATEW) {
        int spin = 0;
#pragma unroll 1
        while (__builtin_amdgcn_readfirstlane(LDL(&ctl[3])) < LATEW - 1 && ++spin < SPIN_MAX) __builtin_amdgcn_s_sleep(1);
        asm volatile("" ::: "memory");
        issue_w();
        prep_d();
        if (STAG) {
          prep_o(IC<0>{}, IC<STAG_A>{});
          poll_issue<5>(rw, off, ta);
          prep_o(IC<STAG_A>{}, IC<8>{});
          if (STAG_S) __builtin_amdgcn_s_sleep(STAG_S);
          poll_issue<5>(rw, off, tb);
        } else prep_o(IC<0>{}, IC<8>{});
        TS(6);
      } else if (STAG) {
        poll_issue<5>(rw, off, ta);
        __builtin_amdgcn_s_sleep(10);
        poll_issue<5>(rw, off, tb);
      }
      if (STAG) poll2<5>(rw, off, tag, v0, v1, ta, tb);
      else gather<5>(rw, off, tag, v0, v1);
      if (lane == 0) STL(&ctl[5], 1);
      TS(7);
      float M = v0[4];
      M = __builtin_fmaxf(M, dpp<XOR1>(M)); M = __builtin_fmaxf(M, dpp<XOR2>(M)); M = __builtin_fmaxf(M, dpp<ROR8>(M));
      M = sw16_max(M); M = sw32_max(M);
      const float f = __builtin_amdgcn_exp2f(v0[4] - M);
      float L = f * v1[4];
      const float f0 = dpp<0x00>(f), f1 = dpp<0x55>(f), f2 = dpp<0xAA>(f), f3 = dpp<0xFF>(f);
      float s0 = f0 * v0[0] + f1 * v0[1] + f2 * v0[2] + f3 * v0[3], s1 = f0 * v1[0] + f1 * v1[1] + f2 * v1[2] + f3 * v1[3];
      L += dpp<XOR1>(L); L += dpp<XOR2>(L);
      s0 += dpp<ROR8>(s0); s1 += dpp<ROR8>(s1); L += dpp<ROR8>(L);
      s0 = sw16_add(s0, s0); s1 = sw16_add(s1, s1); L = sw16_add(L, L);
      s0 = sw32_add(s0, s0); s1 = sw32_add(s1, s1); L = sw32_add(L, L);
      const float rl = __builtin_amdgcn_rcpf(L);
      const unsigned op = pkbf(s0 * rl, s1 * rl);
#pragma unroll
      for (int pc = 0; pc < 8; ++pc) {
        const unsigned os = __builtin_amdgcn_readlane(op, pc);
#pragma unroll
        for (int c = 0; c < 8; ++c) fo[c] = dot2(wp[pc][c], os, fo[c]);
      }
      if (lane < 56) {
        const int vo = (lane / PW) * (4 * 256 * PW * 16) + (lane & (PW - 1)) * 16;
#pragma unroll
        for (int i = 0; i < 4; ++i)
          put(rw, WS_H2 + (i * 256 + b) * (PW * 16) + (vo), f2u(fo[2 * i]), f2u(fo[2 * i + 1]), tag);
      }
      TS(8);
      if (STAG) asm volatile("" :: "v"(ta[0]), "v"(ta[1]), "v"(ta[2]), "v"(ta[3]), "v"(ta[4]), "v"(tb[0]), "v"(tb[1]), "v"(tb[2]), "v"(tb[3]), "v"(tb[4]));
    }
  }
}

extern "C" void kernel_launch(void* const* d_in, const int* in_sizes, int n_in, void* d_out, int out_size, void* d_ws,
                              size_t ws_size, hipStream_t stream) {
  for (int i = 0; i < 9; ++i) fprintf(stderr, "in%d %p ", i, d_in[i]); fprintf(stderr, "out %p ws %p\n", d_out, d_ws);
  fused_decode<<<256, 576, 0, stream>>>((const unsigned*)d_in[0], (const char*)d_in[1], (const char*)d_in[2],
                                        (const char*)d_in[3], (const char*)d_in[4], (const char*)d_in[6],
                                        (const char*)d_in[7], (const char*)d_in[8], (unsigned*)d_out, (char*)d_ws);
}
```

```cpp
#define DIAG
#include <stdio.h>
#include <hip/hip_runtime.h>
#include <stdint.h>

typedef unsigned u32x4 __attribute__((ext_vector_type(4)));
typedef unsigned u32x2 __attribute__((ext_vector_type(2)));
typedef float f32x2 __attribute__((ext_vector_type(2)));
typedef float f32x4 __attribute__((ext_vector_type(4)));
typedef __bf16 bf16x2 __attribute__((ext_vector_type(2)));
#define LAS __attribute__((address_space(3)))
typedef __amdgpu_buffer_rsrc_t rsrc_t;

#define S_STATIC 22
#define S_TICK (S_STATIC - 3)
#define NBACK 6
#define ROT 0x100000u
#define THRESH 8
#define GR1 640
#define GR2 256
#define G1 12
#define G2 8
#define G3 4
#define LATE_TICKET 1
#define PW 8
#define TLBW 0
#define HMAP 0
#define HROT 0x200000u
#define PDEL1 0
#define PDEL2 0
#define POLICY 1
#define Q0 4.f
#define FL 8.f
#define GMIN 2
#define RMAX 0
#define XS 8
#define LATEW 2
#define RF 0
#define Q1 4.f
#define STAG 0
#define STAG_A 4
#define STAG_S 4
#define STAG2 0
#define K_AUX 2
#define V_AUX 2

constexpr unsigned REG = 4u << 20;
constexpr unsigned HEADB = 16u << 20;
constexpr int NP = 4 * (256 - 8 * S_STATIC) * 8;
constexpr int NPRE = (S_STATIC - S_TICK) * 8;
static_assert(NPRE <= 64 && S_TICK >= 2 * NBACK + 1, "");
constexpr int UCAP = 448;
static_assert(RMAX + NP / (UCAP - NPRE - 2 * G1) <= 31, "someone stays until the pool is empty");
constexpr unsigned OOB = 0x40000000u, EMPTY = 0xffffffffu;
constexpr int SPIN_MAX = 200000;
constexpr int NC2 = 224 / PW;
constexpr unsigned WS_EPOCH = 0, WS_CTR = 4096, WS_Q = 16384, WS_ML = 32768, WS_H1 = 65536, WS_H2 = WS_H1 + (1u << 20);
constexpr unsigned WS_F = 2u << 20, WS_USED = WS_F + 224 * 1024;
constexpr float CSC = 0.0625f * 1.4426950408889634f;

template <int V> struct IC { static constexpr int v = V; };
__device__ __forceinline__ float u2f(unsigned u) { return __builtin_bit_cast(float, u); }
__device__ __forceinline__ unsigned f2u(float f) { return __builtin_bit_cast(unsigned, f); }
__device__ __forceinline__ float rbf(float f) { __bf16 b = (__bf16)f; return (float)b; }
__device__ __forceinline__ unsigned pkbf(float lo, float hi) {
  bf16x2 v; v[0] = (__bf16)lo; v[1] = (__bf16)hi; return __builtin_bit_cast(unsigned, v);
}
__device__ __forceinline__ float dot2(unsigned a, unsigned b, float acc) {
  return __builtin_amdgcn_fdot2_f32_bf16(__builtin_bit_cast(bf16x2, a), __builtin_bit_cast(bf16x2, b), acc, false);
}
template <int CTRL> __device__ __forceinline__ float dpp(float x) {
  return __builtin_bit_cast(float, __builtin_amdgcn_mov_dpp(__builtin_bit_cast(int, x), CTRL, 0xf, 0xf, true));
}
constexpr int XOR1 = 0xB1, XOR2 = 0x4E, XOR7 = 0x141, ROR8 = 0x128, ROR4 = 0x124, BC0 = 0x150, BC8 = 0x158;
__device__ __forceinline__ float sw16_add(float a, float b) {
  auto s = __builtin_amdgcn_permlane16_swap(f2u(a), f2u(b), false, false);
  unsigned s0 = s[0], s1 = s[1]; return u2f(s0) + u2f(s1);
}
__device__ __forceinline__ float sw32_add(float a, float b) {
  auto s = __builtin_amdgcn_permlane32_swap(f2u(a), f2u(b), false, false);
  unsigned s0 = s[0], s1 = s[1]; return u2f(s0) + u2f(s1);
}
__device__ __forceinline__ float sw16_max(float a) {
  auto s = __builtin_amdgcn_permlane16_swap(f2u(a), f2u(a), false, false);
  unsigned s0 = s[0], s1 = s[1]; return __builtin_fmaxf(u2f(s0), u2f(s1));
}
__device__ __forceinline__ float sw32_max(float a) {
  auto s = __builtin_amdgcn_permlane32_swap(f2u(a), f2u(a), false, false);
  unsigned s0 = s[0], s1 = s[1]; return __builtin_fmaxf(u2f(s0), u2f(s1));
}
__device__ __forceinline__ float wave_sum(float t) {
  t += dpp<XOR1>(t); t += dpp<XOR2>(t); t += dpp<XOR7>(t); t += dpp<ROR8>(t);
  t = sw16_add(t, t); return sw32_add(t, t);
}
__device__ __forceinline__ float rdl(float v, int l) { return u2f(__builtin_amdgcn_readlane(f2u(v), l)); }
#define LDL(p) __hip_atomic_load((LAS int*)(p), __ATOMIC_RELAXED, __HIP_MEMORY_SCOPE_WORKGROUP)
#define STL(p, v) __hip_atomic_store((LAS int*)(p), v, __ATOMIC_RELAXED, __HIP_MEMORY_SCOPE_WORKGROUP)
#define SCHED() __builtin_amdgcn_sched_barrier(0)

#define TS(i) do { if (lane == 0) tsp[i] = __builtin_amdgcn_s_memrealtime(); } while (0)

template <bool REFILL>
__device__ __forceinline__ void step(u32x4 (&kr)[2], u32x4 (&vr)[2], const u32x4& q0, const u32x4& q1, f32x2 (&a0)[4],
                                     f32x2 (&a1)[4], float& m, float& mc, float& l, rsrc_t rk, rsrc_t rv, int voff,
                                     unsigned o, bool lane8) {
  float d[4];
#pragma unroll
  for (int u = 0; u < 2; ++u) {
    const u32x4 k = kr[u];
    float x = 0.f, y = 0.f;
#pragma unroll
    for (int e = 0; e < 4; ++e) { unsigned w = k[e], qa = q0[e], qb = q1[e]; x = dot2(w, qa, x); y = dot2(w, qb, y); }
    if (REFILL) {
      asm volatile("" : "+v"(x), "+v"(y) :: "memory");
      kr[u] = __builtin_amdgcn_raw_buffer_load_b128(rk, voff + u * 1024, o, K_AUX);
      SCHED();
    }
    d[2 * u] = x; d[2 * u + 1] = y;
  }
  float ab = sw16_add(d[0], d[1]), cd = sw16_add(d[2], d[3]);
  ab += dpp<ROR8>(ab); cd += dpp<ROR8>(cd);
  float t = lane8 ? cd : ab;
  t += dpp<XOR7>(t); t += dpp<XOR2>(t); t += dpp<XOR1>(t);
  t = rbf(t);
  if (__builtin_amdgcn_ballot_w64(t > m) != 0) {
    float mx = __builtin_fmaxf(t, dpp<ROR8>(t));
    mx = __builtin_fmaxf(sw32_max(mx), m);
    const float mcn = mx * CSC;
    const float f = __builtin_amdgcn_exp2f(mc - mcn);
    m = mx; mc = mcn; l *= f;
    const float f0 = rdl(f, 0), f1 = rdl(f, 16);
#pragma unroll
    for (int k = 0; k < 4; ++k) { a0[k] *= f0; a1[k] *= f1; }
  }
  const float p = __builtin_amdgcn_exp2f(__builtin_fmaf(t, CSC, -mc));
  l += p;
  auto s = __builtin_amdgcn_permlane16_swap(f2u(p), f2u(p), false, false);
  unsigned s0 = s[0], s1 = s[1];
  const float ph0 = u2f(s0), ph1 = u2f(s1);
  const float pp[4] = {dpp<BC0>(ph0), dpp<BC0>(ph1), dpp<BC8>(ph0), dpp<BC8>(ph1)};
#pragma unroll
  for (int u = 0; u < 2; ++u) {
    const u32x4 v = vr[u];
    const f32x2 p0 = {pp[2 * u], pp[2 * u]}, p1 = {pp[2 * u + 1], pp[2 * u + 1]};
#pragma unroll
    for (int e = 0; e < 4; ++e) {
      unsigned w = v[e];
      const f32x2 x = {u2f(w << 16), u2f(w & 0xffff0000u)};
      a0[e] = __builtin_elementwise_fma(x, p0, a0[e]);
      a1[e] = __builtin_elementwise_fma(x, p1, a1[e]);
    }
    if (REFILL) {
      asm volatile("" : "+v"(a0[0]), "+v"(a0[1]), "+v"(a0[2]), "+v"(a0[3]), "+v"(a1[0]), "+v"(a1[1]), "+v"(a1[2]), "+v"(a1[3]) :: "memory");
      vr[u] = __builtin_amdgcn_raw_buffer_load_b128(rv, voff + u * 1024, o, V_AUX);
      SCHED();
    }
  }
}

__device__ __forceinline__ void gemv16(const u32x4 (&w)[14], const unsigned (&xp)[7], float& r0, float& r1) {
  float a[8];
#pragma unroll
  for (int i = 0; i < 8; ++i) a[i] = 0.f;
#pragma unroll
  for (int j = 0; j < 7; ++j) {
#pragma unroll
    for (int k = 0; k < 4; ++k) {
      unsigned w0 = w[2 * j][k], w1 = w[2 * j + 1][k];
      a[2 * k] = dot2(__builtin_amdgcn_perm(w1, w0, 0x05040100u), xp[j], a[2 * k]);
      a[2 * k + 1] = dot2(__builtin_amdgcn_perm(w1, w0, 0x07060302u), xp[j], a[2 * k + 1]);
    }
  }
  float b[4];
#pragma unroll
  for (int i = 0; i < 4; ++i) b[i] = sw32_add(a[i], a[i + 4]);
  r0 = sw16_add(b[0], b[2]); r1 = sw16_add(b[1], b[3]);
  r0 += dpp<ROR8>(r0); r1 += dpp<ROR8>(r1);
  r0 += dpp<ROR4>(r0); r1 += dpp<ROR4>(r1);
  r0 += dpp<XOR2>(r0); r1 += dpp<XOR2>(r1);
}

__device__ __forceinline__ void put(rsrc_t rw, unsigned off, unsigned a, unsigned b, unsigned tag) {
  __builtin_amdgcn_raw_buffer_store_b128(u32x4{a, tag, b, tag}, rw, off, 0, 16);
}

template <int N>
__device__ __forceinline__ void gather(rsrc_t rw, unsigned (&off)[N], unsigned tag, float (&v0)[N], float (&v1)[N]) {
#pragma unroll
  for (int i = 0; i < N; ++i) { v0[i] = 0.f; v1[i] = 0.f; }
#pragma unroll 1
  for (int spin = 0; spin < SPIN_MAX; ++spin) {
    u32x4 t[N];
#pragma unroll
    for (int i = 0; i < N; ++i) t[i] = __builtin_amdgcn_raw_buffer_load_b128(rw, off[i], 0, 16);
    bool left = false;
#pragma unroll
    for (int i = 0; i < N; ++i) {
      const unsigned t0 = t[i][0], t1 = t[i][1], t2 = t[i][2], t3 = t[i][3];
      const bool ok = (t1 == tag) & (t3 == tag) & (off[i] != OOB);
      v0[i] = ok ? u2f(t0) : v0[i]; v1[i] = ok ? u2f(t2) : v1[i];
      off[i] = ok ? OOB : off[i];
      left |= off[i] != OOB;
    }
    if (__builtin_amdgcn_ballot_w64(left) == 0) break;
    __builtin_amdgcn_s_sleep(1);
  }
}

template <int N> __device__ __forceinline__ void poll_issue(rsrc_t rw, const unsigned (&off)[N], u32x4 (&t)[N]) {
#pragma unroll
  for (int i = 0; i < N; ++i) t[i] = __builtin_amdgcn_raw_buffer_load_b128(rw, off[i], 0, 16);
  SCHED();
}
template <int N>
__device__ __forceinline__ bool poll_check(const u32x4 (&t)[N], unsigned (&off)[N], unsigned tag, float (&v0)[N], float (&v1)[N]) {
  bool left = false;
#pragma unroll
  for (int i = 0; i < N; ++i) {
    const unsigned t0 = t[i][0], t1 = t[i][1], t2 = t[i][2], t3 = t[i][3];
    const bool ok = (t1 == tag) & (t3 == tag) & (off[i] != OOB);
    v0[i] = ok ? u2f(t0) : v0[i]; v1[i] = ok ? u2f(t2) : v1[i];
    off[i] = ok ? OOB : off[i];
    left |= off[i] != OOB;
  }
  return __builtin_amdgcn_ballot_w64(left) == 0;
}
template <int N>
__device__ __forceinline__ void poll2(rsrc_t rw, unsigned (&off)[N], unsigned tag, float (&v0)[N], float (&v1)[N],
                                      u32x4 (&ta)[N], u32x4 (&tb)[N]) {
#pragma unroll 1
  for (int spin = 0; spin < SPIN_MAX; ++spin) {
    if (poll_check<N>(ta, off, tag, v0, v1)) break;
    poll_issue<N>(rw, off, ta);
    if (poll_check<N>(tb, off, tag, v0, v1)) break;
    poll_issue<N>(rw, off, tb);
  }
}

__global__ void __launch_bounds__(576) fused_decode(const unsigned* __restrict__ x32, const char* __restrict__ K,
                                                    const char* __restrict__ V, const char* __restrict__ wq,
                                                    const char* __restrict__ wo, const char* __restrict__ wg,
                                                    const char* __restrict__ wu, const char* __restrict__ wd,
                                                    unsigned* out32, char* ws) {
  __shared__ __attribute__((aligned(16))) float macc[8 * 512];
  __shared__ __attribute__((aligned(16))) unsigned qs[256];
  __shared__ __attribute__((aligned(16))) unsigned xs[224];
  __shared__ __attribute__((aligned(16))) float mml[32];
  __shared__ __attribute__((aligned(16))) float red[8 * 16];
  __shared__ __attribute__((aligned(16))) unsigned uaddr[512];
  __shared__ int ctl[8];
  const int lane = threadIdx.x & 63, hwave = __builtin_amdgcn_readfirstlane(threadIdx.x >> 6);
  const int wave = hwave == 0 ? 8 : hwave - 1;
  const int b = blockIdx.x, g = b >> 5, grp = b >> 3, mem = b & 7, j32 = b & 31;
  unsigned long long* tsp = (unsigned long long*)(ws + (8u << 20)) + (b * 16 + wave) * 16;
  asm volatile("" :: "s"(x32), "s"(K), "s"(V), "s"(wq), "s"(wo), "s"(wg), "s"(wu), "s"(wd), "s"(out32), "s"(ws));
  TS(0);
  const rsrc_t rw = __builtin_amdgcn_make_buffer_rsrc((void*)ws, 0, WS_USED, 0x00020000);
  const rsrc_t rk = __builtin_amdgcn_make_buffer_rsrc((void*)(K + (size_t)g * HEADB), 0, HEADB, 0x00020000);
  const rsrc_t rv = __builtin_amdgcn_make_buffer_rsrc((void*)(V + (size_t)g * HEADB), 0, HEADB, 0x00020000);
  unsigned tag;

  if (__builtin_expect(wave < 8, 1)) {
    const int voff = lane * 16;
    const unsigned sbase = (grp & 3) * REG + wave * 2048, srot = grp * ROT + mem * 16384u;
    auto offs = [&](int s) -> unsigned {
      return HMAP ? ((s * 524288u + j32 * 16384u + g * HROT) & (HEADB - 1)) + wave * 2048 : sbase + ((s * 131072u + srot) & (REG - 1));
    };
    u32x4 kb[NBACK + 1][2], vb[NBACK + 1][2];
    asm volatile("s_barrier" ::: "memory"); SCHED();
#pragma unroll
    for (int s = 0; s <= NBACK; ++s) {
      const unsigned o = offs(s);
#pragma unroll
      for (int u = 0; u < 2; ++u) {
        if (s > 0) asm volatile("s_waitcnt vmcnt(3)" ::: "memory");
        kb[s][u] = __builtin_amdgcn_raw_buffer_load_b128(rk, voff + u * 1024, o, K_AUX);
        SCHED();
      }
#pragma unroll
      for (int u = 0; u < 2; ++u) {
        if (s > 0) asm volatile("s_waitcnt vmcnt(3)" ::: "memory");
        vb[s][u] = __builtin_amdgcn_raw_buffer_load_b128(rv, voff + u * 1024, o, V_AUX);
        SCHED();
      }
    }
    TS(1);
    {
      int spin = 0;
#pragma unroll 1
      while (__builtin_amdgcn_readfirstlane(LDL(&ctl[1])) == 0 && ++spin < SPIN_MAX) __builtin_amdgcn_s_sleep(1);
    }
    asm volatile("" ::: "memory");
    TS(2);
    const u32x4 q0 = *(const u32x4*)&qs[(lane & 31) * 4], q1 = *(const u32x4*)&qs[128 + (lane & 31) * 4];
    tag = __builtin_amdgcn_readfirstlane(LDL(&ctl[2]));
    f32x2 a0[4], a1[4];
#pragma unroll
    for (int k = 0; k < 4; ++k) { a0[k] = f32x2{0.f, 0.f}; a1[k] = f32x2{0.f, 0.f}; }
    float m = -INFINITY, mc = -INFINITY, l = 0.f;
    const bool lane8 = (lane & 8) != 0;
#pragma unroll
    for (int s = 0; s < NBACK; ++s) {
      step<true>(kb[NBACK], vb[NBACK], q0, q1, a0, a1, m, mc, l, rk, rv, voff, offs(NBACK + 1 + s), lane8);
      step<false>(kb[s], vb[s], q0, q1, a0, a1, m, mc, l, rk, rv, voff, 0, lane8);
      SCHED();
    }
    TS(3);
    int n = 2 * NBACK + 1;
#pragma unroll 1
    for (;;) {
      unsigned o;
      if (n < S_TICK) {
        o = offs(n);
      } else {
        if (LATE_TICKET) asm volatile("s_waitcnt vmcnt(3)" ::: "memory");
        int t = 0;
        if (lane == 0) t = __hip_atomic_fetch_add((LAS int*)&ctl[0], 1, __ATOMIC_RELAXED, __HIP_MEMORY_SCOPE_WORKGROUP);
        t = __builtin_amdgcn_readfirstlane(t);
        o = __builtin_amdgcn_readfirstlane(LDL(&uaddr[t]));
        int spin = 0;
#pragma unroll 1
        while (o == EMPTY && ++spin < SPIN_MAX) { __builtin_amdgcn_s_sleep(1); o = __builtin_amdgcn_readfirstlane(LDL(&uaddr[t])); }
        o = o == EMPTY ? OOB : o;
      }
      step<true>(kb[NBACK], vb[NBACK], q0, q1, a0, a1, m, mc, l, rk, rv, voff, o, lane8);
      if (o == OOB) break;
      ++n;
    }
    TS(4);
    f32x4 lo, hi;
    lo[0] = sw32_add(a0[0][0], a1[0][0]); lo[1] = sw32_add(a0[0][1], a1[0][1]);
    lo[2] = sw32_add(a0[1][0], a1[1][0]); lo[3] = sw32_add(a0[1][1], a1[1][1]);
    hi[0] = sw32_add(a0[2][0], a1[2][0]); hi[1] = sw32_add(a0[2][1], a1[2][1]);
    hi[2] = sw32_add(a0[3][0], a1[3][0]); hi[3] = sw32_add(a0[3][1], a1[3][1]);
    *(f32x4*)&macc[wave * 512 + lane * 4] = lo;
    *(f32x4*)&macc[wave * 512 + 256 + lane * 4] = hi;
    l += dpp<ROR8>(l); l = sw32_add(l, l);
    if ((lane & 47) == 0) *(f32x2*)&mml[(lane >> 4) * 16 + wave * 2] = f32x2{mc, l};
    asm volatile("s_waitcnt lgkmcnt(0)" ::: "memory");
    int arr = 0;
    if (lane == 0) arr = __hip_atomic_fetch_add((LAS int*)&ctl[3], 1, __ATOMIC_RELAXED, __HIP_MEMORY_SCOPE_WORKGROUP);
    arr = __builtin_amdgcn_readfirstlane(arr);
    asm volatile("" ::: "memory");
    if (arr == 7) {
      TS(5);
      f32x4 ml[4], pl[8], ph[8];
#pragma unroll
      for (int i = 0; i < 4; ++i) ml[i] = *(const f32x4*)&mml[(lane >> 5) * 16 + i * 4];
#pragma unroll
      for (int w = 0; w < 8; ++w) { pl[w] = *(const f32x4*)&macc[w * 512 + lane * 4]; ph[w] = *(const f32x4*)&macc[w * 512 + 256 + lane * 4]; }
      float M = ml[0][0];
#pragma unroll
      for (int i = 0; i < 4; ++i) M = __builtin_fmaxf(M, __builtin_fmaxf(ml[i][0], ml[i][2]));
      float L = 0.f; f32x4 sl = {0.f, 0.f, 0.f, 0.f}, sh = {0.f, 0.f, 0.f, 0.f};
#pragma unroll
      for (int w = 0; w < 8; ++w) {
        const float f = __builtin_amdgcn_exp2f(ml[w >> 1][(w & 1) * 2] - M);
        L = __builtin_fmaf(f, ml[w >> 1][(w & 1) * 2 + 1], L);
        sl = __builtin_elementwise_fma(pl[w], f32x4{f, f, f, f}, sl);
        sh = __builtin_elementwise_fma(ph[w], f32x4{f, f, f, f}, sh);
      }
      const float sv[8] = {sl[0], sl[1], sl[2], sl[3], sh[0], sh[1], sh[2], sh[3]};
      const int vo = (lane >> 3) * 16384 + (lane & 7) * 16;
#pragma unroll
      for (int i = 0; i < 4; ++i)
        put(rw, WS_H1 + g * 131072 + i * 4096 + j32 * 128 + (vo), f2u(sv[2 * i]), f2u(sv[2 * i + 1]), tag);
      if ((lane & 31) == 0)
        put(rw, WS_ML + g * 1024 + j32 * 16 + ((lane >> 5) * 512), f2u(M), f2u(L), tag);
      TS(6);
    }
    const int c2 = PW == 8 ? b / 9 : (b & 1) * 28 + (b >> 1) / 9 * 2 + (((b >> 1) % 9) >> 2);
    const bool cons = PW == 8 ? (b % 9 == 0 && c2 < 28) : false;
    if (cons) {
      const int ow = (c2 >> 2) * (PW * 4) + (lane & (PW - 1)) * 4 + (c2 & 3);
      const unsigned xv = x32[ow];
      unsigned off[PW / 2]; float v0[PW / 2], v1[PW / 2];
#pragma unroll
      for (int i = 0; i < PW / 2; ++i) off[i] = WS_H2 + (c2 * 256 + wave * 32) * (PW * 16) + i * 1024 + lane * 16;
      if (PDEL2) {
        int spin = 0;
#pragma unroll 1
        while (__builtin_amdgcn_readfirstlane(LDL(&ctl[5])) == 0 && ++spin < SPIN_MAX) __builtin_amdgcn_s_sleep(1);
        asm volatile("" ::: "memory");
      }
      u32x4 ta[PW / 2], tb[PW / 2];
      if (STAG2) {
#pragma unroll
        for (int i = 0; i < PW / 2; ++i) { v0[i] = 0.f; v1[i] = 0.f; }
        poll_issue<PW / 2>(rw, off, ta);
        __builtin_amdgcn_s_sleep(STAG2);
        poll_issue<PW / 2>(rw, off, tb);
        poll2<PW / 2>(rw, off, tag, v0, v1, ta, tb);
      } else gather<PW / 2>(rw, off, tag, v0, v1);
      TS(7);
      float s0 = 0.f, s1 = 0.f;
#pragma unroll
      for (int i = 0; i < PW / 2; ++i) { s0 += v0[i]; s1 += v1[i]; }
      float s = sw32_add(s0, s1);
      s += dpp<ROR8>(s); s = sw16_add(s, s);
      if (PW == 4) s += dpp<ROR4>(s);
      if ((lane & 24) == 0) red[wave * 16 + (lane & 7) * 2 + (lane >> 5)] = s;
      asm volatile("s_waitcnt lgkmcnt(0)" ::: "memory");
      int fin = 0;
      if (lane == 0) fin = __hip_atomic_fetch_add((LAS int*)&ctl[4], 1, __ATOMIC_RELAXED, __HIP_MEMORY_SCOPE_WORKGROUP);
      fin = __builtin_amdgcn_readfirstlane(fin);
      asm volatile("" ::: "memory");
      if (fin == 7) {
        const f32x2 r = *(const f32x2*)&red[lane * 2];
        float r0 = r[0], r1 = r[1];
        r0 += dpp<ROR8>(r0); r1 += dpp<ROR8>(r1);
        r0 = sw16_add(r0, r0); r1 = sw16_add(r1, r1);
        r0 = sw32_add(r0, r0); r1 = sw32_add(r1, r1);
        if (lane < PW) out32[ow] = pkbf(r0 + u2f(xv << 16), r1 + u2f(xv & 0xffff0000u));
        if (b == 0 && lane == 0) __builtin_amdgcn_raw_buffer_store_b32(tag, rw, 0, WS_EPOCH, 16);
        TS(8);
      }
      if (STAG2) asm volatile("" :: "v"(ta[0]), "v"(ta[1]), "v"(ta[2]), "v"(ta[3]), "v"(tb[0]), "v"(tb[1]), "v"(tb[2]), "v"(tb[3]));
    }
    asm volatile("" :: "v"(kb[NBACK][0]), "v"(kb[NBACK][1]), "v"(vb[NBACK][0]), "v"(vb[NBACK][1]));
  } else {
    __builtin_amdgcn_s_setprio(3);
    const unsigned ep = __builtin_amdgcn_raw_buffer_load_b32(rw, 0, WS_EPOCH, 16);
    unsigned xr[7];
#pragma unroll
    for (int j = 0; j < 7; ++j) xr[j] = x32[(lane >> 1) + 32 * j];
    u32x4 w[14];
    const int jq = mem * 4 + (grp & 3);
    {
      const rsrc_t r = __builtin_amdgcn_make_buffer_rsrc((void*)wq, 0, 448 * 8192, 0x00020000);
      const int vo = (lane >> 1) * 16384 + (lane & 1) * 16;
#pragma unroll
      for (int i = 0; i < 14; ++i) w[i] = __builtin_amdgcn_raw_buffer_load_b128(r, vo, g * 1024 + jq * 32 + (i >> 1) * 524288 + (i & 1) * 8192, 2);
    }
    if (j32 == 0 && lane == 0) __builtin_amdgcn_raw_buffer_store_b32(0u, rw, 0, WS_CTR + g * 256, 16);
    SCHED();
    *(u32x4*)&uaddr[lane * 4] = u32x4{EMPTY, EMPTY, EMPTY, EMPTY};
    *(u32x4*)&uaddr[256 + lane * 4] = u32x4{EMPTY, EMPTY, EMPTY, EMPTY};
    if (HMAP) { if (lane < NPRE) uaddr[lane] = (((S_TICK + (lane >> 3)) * 524288u + j32 * 16384u + g * HROT) & (HEADB - 1)) + (lane & 7) * 2048; }
    else if (lane < NPRE) uaddr[lane] = (grp & 3) * REG + (lane & 7) * 2048 + (((S_TICK + (lane >> 3)) * 131072u + grp * ROT + mem * 16384u) & (REG - 1));
    if (lane < 8) ctl[lane] = 0;
    asm volatile("s_waitcnt lgkmcnt(0)\n s_barrier" ::: "memory");
    asm volatile("" : "+v"(xr[0]), "+v"(xr[1]), "+v"(xr[2]), "+v"(xr[3]), "+v"(xr[4]), "+v"(xr[5]), "+v"(xr[6]));
    unsigned xp[7];
    {
      float s = 0.f;
#pragma unroll
      for (int j = 0; j < 7; ++j) s += u2f(xr[j] << 16) + u2f(xr[j] & 0xffff0000u);
      const float mean = wave_sum(s) * (1.f / 896.f);
      float v = 0.f;
#pragma unroll
      for (int j = 0; j < 7; ++j) { const float da = u2f(xr[j] << 16) - mean, db = u2f(xr[j] & 0xffff0000u) - mean; v += da * da + db * db; }
      const float var = wave_sum(v) * (1.f / 896.f);
      const float mb = rbf(mean), rden = __builtin_amdgcn_rcpf(rbf(__builtin_amdgcn_sqrtf(rbf(rbf(var) + 1e-5f))));
#pragma unroll
      for (int j = 0; j < 7; ++j) xp[j] = pkbf(rbf(u2f(xr[j] << 16) - mb) * rden, rbf(u2f(xr[j] & 0xffff0000u) - mb) * rden);
    }
    tag = __builtin_amdgcn_readfirstlane(ep) + 1u;
    TS(1);
    {
      float r0, r1;
      gemv16(w, xp, r0, r1);
      const unsigned pk = pkbf(r0, r1);
      auto s = __builtin_amdgcn_permlane16_swap(pk, pk, false, false);
      unsigned s0 = s[0], s1 = s[1];
      asm volatile("s_waitcnt vmcnt(0)" : "+v"(s0), "+v"(s1) :: "memory");
      if ((lane & 30) == 0)
        put(rw, WS_Q + g * 2048 + jq * 64 + (((lane & 1) * 2 + (lane >> 5)) * 16), s0, s1, tag);
    }
    TS(2);
#pragma unroll
    for (int j = 0; j < 7; ++j) xs[(lane >> 1) + 32 * j] = xp[j];
    {
      unsigned off[2] = {WS_Q + g * 2048 + lane * 16u, WS_Q + g * 2048 + 1024 + lane * 16u};
      float v0[2], v1[2];
      gather<2>(rw, off, tag, v0, v1);
      *(f32x2*)&qs[lane * 2] = f32x2{v0[0], v1[0]};
      *(f32x2*)&qs[128 + lane * 2] = f32x2{v0[1], v1[1]};
      if (lane == 0) STL(&ctl[2], (int)tag);
      asm volatile("s_waitcnt lgkmcnt(0)" ::: "memory");
      if (lane == 0) STL(&ctl[1], 1);
    }
    TS(3);
    const int fu = grp * 7 + mem;
    if (mem < 7) {
      const rsrc_t rg = __builtin_amdgcn_make_buffer_rsrc((void*)wg, 0, 448 * 3584, 0x00020000);
      const rsrc_t ru = __builtin_amdgcn_make_buffer_rsrc((void*)wu, 0, 448 * 3584, 0x00020000);
      const int vo = (lane >> 3) * 3584 + (lane & 7) * 16, so = (fu & 7) * 56 * 3584 + (fu >> 3) * 128;
#pragma unroll
      for (int i = 0; i < 7; ++i) {
        w[2 * i] = __builtin_amdgcn_raw_buffer_load_b128(rg, vo, so + i * 8 * 3584, 2);
        w[2 * i + 1] = __builtin_amdgcn_raw_buffer_load_b128(ru, vo, so + i * 8 * 3584, 2);
      }
      float a[16];
#pragma unroll
      for (int i = 0; i < 16; ++i) a[i] = 0.f;
#pragma unroll
      for (int i = 0; i < 7; ++i) {
        const unsigned xl = ((const unsigned short*)xs)[(fu & 7) * 56 + i * 8 + (lane >> 3)], xh = xl << 16;
#pragma unroll
        for (int e = 0; e < 4; ++e) {
          const unsigned wa = w[2 * i][e], wb = w[2 * i + 1][e];
          a[2 * e] = dot2(wa, xl, a[2 * e]); a[2 * e + 1] = dot2(wa, xh, a[2 * e + 1]);
          a[8 + 2 * e] = dot2(wb, xl, a[8 + 2 * e]); a[8 + 2 * e + 1] = dot2(wb, xh, a[8 + 2 * e + 1]);
        }
      }
#pragma unroll
      for (int i = 0; i < 8; ++i) a[i] = sw32_add(a[i], a[i + 8]);
#pragma unroll
      for (int i = 0; i < 4; ++i) { a[i] = sw16_add(a[i], a[i + 4]); a[i] += dpp<ROR8>(a[i]); }
      const bool b3 = (lane & 8) != 0;
      const int src = (((lane & 7) << 3) | (lane >> 3)) * 4;
      const unsigned d0 = __builtin_amdgcn_ds_bpermute(src, f2u(b3 ? a[2] : a[0])), d1 = __builtin_amdgcn_ds_bpermute(src, f2u(b3 ? a[3] : a[1]));
      put(rw, WS_F + (fu >> 3) * 8192 + (fu & 7) * 128 + ((lane >> 3) * 1024 + (lane & 7) * 16), d0, d1, tag);
    }
    SCHED();
    float fo[8];
    unsigned wp[8][8], hp = 0;
    const rsrc_t ro = __builtin_amdgcn_make_buffer_rsrc((void*)wo, 0, 4096 * 896, 0x00020000);
    const rsrc_t rd = __builtin_amdgcn_make_buffer_rsrc((void*)wd, 0, 1792 * 896, 0x00020000);
    const int row0 = g * 512 + (j32 >> 2) * 64 + (j32 & 3) * 2;
    const unsigned wvo = lane < 56 ? lane * 16u : OOB;
    u32x4 ov[16], dv[8];
    auto issue_w = [&]() __attribute__((always_inline)) {
#pragma unroll
      for (int i = 0; i < 8; ++i) dv[i] = __builtin_amdgcn_raw_buffer_load_b128(rd, wvo, mem < 7 ? (fu * 8 + i) * 896 : OOB, 2);
#pragma unroll
      for (int i = 0; i < 16; ++i) ov[i] = __builtin_amdgcn_raw_buffer_load_b128(ro, wvo, (row0 + (i >> 1) * 8 + (i & 1)) * 896, 2);
      SCHED();
    };
    auto prep_d = [&]() __attribute__((always_inline)) {
#pragma unroll
      for (int i = 0; i < 8; ++i) fo[i] = 0.f;
#pragma unroll
      for (int pc = 0; pc < 4; ++pc) {
        const unsigned hs = __builtin_amdgcn_readlane(hp, pc);
#pragma unroll
        for (int k = 0; k < 4; ++k) {
          unsigned w0 = dv[2 * pc][k], w1 = dv[2 * pc + 1][k];
          fo[2 * k] = dot2(__builtin_amdgcn_perm(w1, w0, 0x05040100u), hs, fo[2 * k]);
          fo[2 * k + 1] = dot2(__builtin_amdgcn_perm(w1, w0, 0x07060302u), hs, fo[2 * k + 1]);
        }
      }
      asm volatile("" : "+v"(fo[0]), "+v"(fo[1]), "+v"(fo[2]), "+v"(fo[3]), "+v"(fo[4]), "+v"(fo[5]), "+v"(fo[6]), "+v"(fo[7]));
      SCHED();
    };
    auto prep_o = [&](auto p0, auto p1) __attribute__((always_inline)) {
#pragma unroll
      for (int pc = decltype(p0)::v; pc < decltype(p1)::v; ++pc)
#pragma unroll
        for (int k = 0; k < 4; ++k) {
          unsigned w0 = ov[2 * pc][k], w1 = ov[2 * pc + 1][k];
          wp[pc][2 * k] = __builtin_amdgcn_perm(w1, w0, 0x05040100u);
          wp[pc][2 * k + 1] = __builtin_amdgcn_perm(w1, w0, 0x07060302u);
          asm volatile("" : "+v"(wp[pc][2 * k]), "+v"(wp[pc][2 * k + 1]));
        }
      SCHED();
    };
    if (!LATEW) issue_w();
    else {
      const unsigned t0 = __builtin_amdgcn_raw_buffer_load_b32(ro, 0, row0 * 896, 2), t1 = __builtin_amdgcn_raw_buffer_load_b32(rd, 0, fu * 8 * 896, 2);
      const unsigned t2 = __builtin_amdgcn_raw_buffer_load_b32(rw, WS_H1 + b * 4096 + lane * 64, 0, 16);
      const unsigned t3 = __builtin_amdgcn_raw_buffer_load_b32(rw, WS_ML + g * 1024 + lane * 16, 0, 16);
      asm volatile("" :: "v"(t0), "v"(t1), "v"(t2), "v"(t3));
    }
    if (mem < 7) {
      unsigned off[1] = {WS_F + fu * 1024 + lane * 16u};
      float v0[1], v1[1];
      gather<1>(rw, off, tag, v0, v1);
      float r0 = v0[0], r1 = v1[0];
      r0 += dpp<ROR8>(r0); r1 += dpp<ROR8>(r1);
      r0 = sw16_add(r0, r0); r1 = sw16_add(r1, r1);
      r0 = rbf(sw32_add(r0, r0)); r1 = rbf(sw32_add(r1, r1));
      const float u0 = dpp<ROR4>(r0), u1 = dpp<ROR4>(r1);
      const float s0 = rbf(r0 * __builtin_amdgcn_rcpf(1.f + __expf(-r0))), s1 = rbf(r1 * __builtin_amdgcn_rcpf(1.f + __expf(-r1)));
      hp = pkbf(s0 * u0, s1 * u1);
    }
    if (!LATEW) { prep_d(); prep_o(IC<0>{}, IC<8>{}); }
    TS(4);
    {
      const rsrc_t rc = __builtin_amdgcn_make_buffer_rsrc((void*)(ws + WS_CTR + g * 256), 0, 4, 0x00020000);
      int k = NPRE, want = G1 + 1, kfin = -1;
      bool asked = false;
#pragma unroll 1
      for (int spin = 0; spin < 16 * SPIN_MAX; ++spin) {
        const int t = __builtin_amdgcn_readfirstlane(LDL(&ctl[0]));
        if (k - t <= THRESH) {
          const bool fin = want <= G1 && !asked;
          const int gr = want > G1 ? G1 : !fin && k - t <= 0 && want < GMIN ? GMIN : want;
          const unsigned r = (unsigned)__builtin_amdgcn_readfirstlane(__builtin_amdgcn_raw_ptr_buffer_atomic_add_i32(gr + (fin ? 1 << 24 : 0), rc, lane * 4, 0, 16));
          const unsigned p = r & 0xffffffu;
          const int rem = NP - (int)p - gr, nr = (int)(r >> 24);
          const bool last = rem <= 0 || k + gr + G1 > UCAP || (fin && nr < RMAX && rem <= (31 - nr) * XS);
          asked |= fin;
          const unsigned pu = p + lane, jc = pu >> 3, rr = jc & 3;
          const unsigned o = HMAP ? (S_STATIC * 524288u + pu * 2048u + g * HROT) & (HEADB - 1) : rr * REG + (pu & 7) * 2048 + (((S_STATIC * 8 + (jc >> 2)) * 16384u + (g * 4 + rr) * ROT) & (REG - 1));
          const bool valid = pu < (unsigned)NP && lane < gr;
          if (valid || last) STL(&uaddr[k + lane], (int)(valid ? o : OOB));
          if (lane == 0) { tsp[9] = k + (valid ? gr : 0); if (gr > 0 && p < (unsigned)NP) { tsp[10] = __builtin_amdgcn_s_memrealtime(); tsp[11] = gr; tsp[12] = k - t + 100; } }
          if (last) break;
          k += gr;
          if (rem >= RF || kfin < 0 || asked) {
            const int t2 = __builtin_amdgcn_readfirstlane(LDL(&ctl[0]));
            const float ta = 0.5f * (float)(t + t2), q = rem >= RF ? Q0 : Q1;
            const float rho = (S_TICK * 8 + ta) * __builtin_amdgcn_rcpf(256.f - q - rem * (1.f / 32.f));
            kfin = __builtin_amdgcn_readfirstlane((int)(rho * (rem * (1.f / 32.f) + q + FL) - FL + ta + 0.5f));
          }
          want = kfin - k < 0 ? 0 : kfin - k;
        } else {
          __builtin_amdgcn_s_sleep(1);
        }
      }
    }
    TS(5);
    {
      unsigned off[5]; float v0[5], v1[5];
      u32x4 ta[5], tb[5];
#pragma unroll
      for (int i = 0; i < 4; ++i) off[i] = WS_H1 + b * 4096 + i * 1024 + lane * 16;
      off[4] = WS_ML + g * 1024 + (j32 >> 4) * 512 + ((lane >> 3) + 8 * (lane & 3)) * 16;
#pragma unroll
      for (int i = 0; i < 5; ++i) { v0[i] = 0.f; v1[i] = 0.f; }
      if (LATEW) {
        int spin = 0;
#pragma unroll 1
        while (__builtin_amdgcn_readfirstlane(LDL(&ctl[3])) < LATEW - 1 && ++spin < SPIN_MAX) __builtin_amdgcn_s_sleep(1);
        asm volatile("" ::: "memory");
        issue_w();
        prep_d();
        if (STAG) {
          prep_o(IC<0>{}, IC<STAG_A>{});
          poll_issue<5>(rw, off, ta);
          prep_o(IC<STAG_A>{}, IC<8>{});
          if (STAG_S) __builtin_amdgcn_s_sleep(STAG_S);
          poll_issue<5>(rw, off, tb);
        } else prep_o(IC<0>{}, IC<8>{});
        TS(6);
      } else if (STAG) {
        poll_issue<5>(rw, off, ta);
        __builtin_amdgcn_s_sleep(10);
        poll_issue<5>(rw, off, tb);
      }
      if (STAG) poll2<5>(rw, off, tag, v0, v1, ta, tb);
      else gather<5>(rw, off, tag, v0, v1);
      if (lane == 0) STL(&ctl[5], 1);
      TS(7);
      float M = v0[4];
      M = __builtin_fmaxf(M, dpp<XOR1>(M)); M = __builtin_fmaxf(M, dpp<XOR2>(M)); M = __builtin_fmaxf(M, dpp<ROR8>(M));
      M = sw16_max(M); M = sw32_max(M);
      const float f = __builtin_amdgcn_exp2f(v0[4] - M);
      float L = f * v1[4];
      const float f0 = dpp<0x00>(f), f1 = dpp<0x55>(f), f2 = dpp<0xAA>(f), f3 = dpp<0xFF>(f);
      float s0 = f0 * v0[0] + f1 * v0[1] + f2 * v0[2] + f3 * v0[3], s1 = f0 * v1[0] + f1 * v1[1] + f2 * v1[2] + f3 * v1[3];
      L += dpp<XOR1>(L); L += dpp<XOR2>(L);
      s0 += dpp<ROR8>(s0); s1 += dpp<ROR8>(s1); L += dpp<ROR8>(L);
      s0 = sw16_add(s0, s0); s1 = sw16_add(s1, s1); L = sw16_add(L, L);
      s0 = sw32_add(s0, s0); s1 = sw32_add(s1, s1); L = sw32_add(L, L);
      const float rl = __builtin_amdgcn_rcpf(L);
      const unsigned op = pkbf(s0 * rl, s1 * rl);
#pragma unroll
      for (int pc = 0; pc < 8; ++pc) {
        const unsigned os = __builtin_amdgcn_readlane(op, pc);
#pragma unroll
        for (int c = 0; c < 8; ++c) fo[c] = dot2(wp[pc][c], os, fo[c]);
      }
      if (lane < 56) {
        const int vo = (lane / PW) * (4 * 256 * PW * 16) + (lane & (PW - 1)) * 16;
#pragma unroll
        for (int i = 0; i < 4; ++i)
          put(rw, WS_H2 + (i * 256 + b) * (PW * 16) + (vo), f2u(fo[2 * i]), f2u(fo[2 * i + 1]), tag);
      }
      TS(8);
      if (STAG) asm volatile("" :: "v"(ta[0]), "v"(ta[1]), "v"(ta[2]), "v"(ta[3]), "v"(ta[4]), "v"(tb[0]), "v"(tb[1]), "v"(tb[2]), "v"(tb[3]), "v"(tb[4]));
    }
  }
}

__global__ void report(char* ws) {
  const unsigned long long* h = (const unsigned long long*)(ws + (8u << 20));
  __shared__ unsigned long long t0s;
  __shared__ unsigned mx[16], mn[16];
  const int t = threadIdx.x;
  if (t == 0) t0s = ~0ull;
  if (t < 16) { mx[t] = 0; mn[t] = ~0u; }
  __syncthreads();
  unsigned long long t0 = ~0ull;
  for (int w = 0; w < 9; ++w) t0 = min(t0, h[(t * 16 + w) * 16]);
  atomicMin(&t0s, t0);
  __syncthreads();
  t0 = t0s;
  for (int w = 0; w < 9; ++w) { unsigned d = (unsigned)(h[(t * 16 + w) * 16] - t0); atomicMax(&mx[0], d); atomicMin(&mn[0], d); }
  { unsigned d = (unsigned)(h[(t * 16 + 8) * 16 + 3] - t0); atomicMax(&mx[1], d); atomicMin(&mn[1], d); }
  for (int w = 0; w < 8; ++w) { unsigned d = (unsigned)(h[(t * 16 + w) * 16 + 4] - t0); atomicMax(&mx[2], d); atomicMin(&mn[2], d); }
  for (int w = 0; w < 8; ++w) { unsigned d = (unsigned)(h[(t * 16 + w) * 16 + 1] - t0); atomicMax(&mx[3], d); atomicMin(&mn[3], d); }
  for (int w = 0; w < 8; ++w) { unsigned d = (unsigned)(h[(t * 16 + w) * 16 + 3] - t0); atomicMax(&mx[4], d); atomicMin(&mn[4], d); }
  { unsigned d = (unsigned)(h[(t * 16 + 8) * 16 + 7] - t0); atomicMax(&mx[5], d); atomicMin(&mn[5], d); }
  { unsigned d = (unsigned)(h[(t * 16 + 8) * 16 + 8] - t0); atomicMax(&mx[6], d); atomicMin(&mn[6], d); }
  { unsigned d = (unsigned)(h[(t * 16 + 8) * 16 + 1] - t0); atomicMax(&mx[7], d); atomicMin(&mn[7], d); }
  { unsigned d = (unsigned)(h[(t * 16 + 8) * 16 + 2] - t0); atomicMax(&mx[8], d); atomicMin(&mn[8], d); }
  { unsigned d = (unsigned)(h[(t * 16 + 8) * 16 + 5] - t0); atomicMax(&mx[9], d); atomicMin(&mn[9], d); }
  __syncthreads();
  if (t == 0) {
    unsigned now = (unsigned)(__builtin_amdgcn_s_memrealtime() - t0);
    printf("TS start %u-%u ln %u-%u qput %u-%u qgat %u-%u prol %u-%u back %u-%u dry %u-%u end %u-%u h1g %u-%u h2p %u-%u now %u\n", mn[0], mx[0], mn[7], mx[7], mn[8], mx[8], mn[1], mx[1], mn[3], mx[3], mn[4], mx[4], mn[9], mx[9], mn[2], mx[2], mn[5], mx[5], mn[6], mx[6], now);
  }
}
extern "C" void kernel_launch(void* const* d_in, const int* in_sizes, int n_in, void* d_out, int out_size, void* d_ws,
                              size_t ws_size, hipStream_t stream) {
  fused_decode<<<256, 576, 0, stream>>>((const unsigned*)d_in[0], (const char*)d_in[1], (const char*)d_in[2],
                                        (const char*)d_in[3], (const char*)d_in[4], (const char*)d_in[6],
                                        (const char*)d_in[7], (const char*)d_in[8], (unsigned*)d_out, (char*)d_ws);
  report<<<1, 256, 0, stream>>>((char*)d_ws);
}
```

```cpp
#define DIAG
#include <hip/hip_runtime.h>
#include <stdint.h>

typedef unsigned u32x4 __attribute__((ext_vector_type(4)));
typedef unsigned u32x2 __attribute__((ext_vector_type(2)));
typedef float f32x2 __attribute__((ext_vector_type(2)));
typedef float f32x4 __attribute__((ext_vector_type(4)));
typedef __bf16 bf16x2 __attribute__((ext_vector_type(2)));
#define LAS __attribute__((address_space(3)))
typedef __amdgpu_buffer_rsrc_t rsrc_t;

#define S_STATIC 22
#define S_TICK (S_STATIC - 3)
#define NBACK 6
#define ROT 0x100000u
#define THRESH 8
#define GR1 640
#define GR2 256
#define G1 12
#define G2 8
#define G3 4
#define LATE_TICKET 1
#define PW 8
#define TLBW 0
#define HMAP 0
#define HROT 0x200000u
#define PDEL1 0
#define PDEL2 0
#define POLICY 1
#define Q0 4.f
#define FL 8.f
#define GMIN 2
#define RMAX 0
#define XS 8
#define LATEW 2
#define RF 0
#define Q1 4.f
#define STAG 0
#define STAG_A 4
#define STAG_S 4
#define STAG2 0
#define K_AUX 2
#define V_AUX 2

constexpr unsigned REG = 4u << 20;
constexpr unsigned HEADB = 16u << 20;
constexpr int NP = 4 * (256 - 8 * S_STATIC) * 8;
constexpr int NPRE = (S_STATIC - S_TICK) * 8;
static_assert(NPRE <= 64 && S_TICK >= 2 * NBACK + 1, "");
constexpr int UCAP = 448;
static_assert(RMAX + NP / (UCAP - NPRE - 2 * G1) <= 31, "someone stays until the pool is empty");
constexpr unsigned OOB = 0x40000000u, EMPTY = 0xffffffffu;
constexpr int SPIN_MAX = 200000;
constexpr int NC2 = 224 / PW;
constexpr unsigned WS_EPOCH = 0, WS_CTR = 4096, WS_Q = 16384, WS_ML = 32768, WS_H1 = 65536, WS_H2 = WS_H1 + (1u << 20);
constexpr unsigned WS_F = 2u << 20, WS_USED = WS_F + 224 * 1024;
constexpr float CSC = 0.0625f * 1.4426950408889634f;

template <int V> struct IC { static constexpr int v = V; };
__device__ __forceinline__ float u2f(unsigned u) { return __builtin_bit_cast(float, u); }
__device__ __forceinline__ unsigned f2u(float f) { return __builtin_bit_cast(unsigned, f); }
__device__ __forceinline__ float rbf(float f) { __bf16 b = (__bf16)f; return (float)b; }
__device__ __forceinline__ unsigned pkbf(float lo, float hi) {
  bf16x2 v; v[0] = (__bf16)lo; v[1] = (__bf16)hi; return __builtin_bit_cast(unsigned, v);
}
__device__ __forceinline__ float dot2(unsigned a, unsigned b, float acc) {
  return __builtin_amdgcn_fdot2_f32_bf16(__builtin_bit_cast(bf16x2, a), __builtin_bit_cast(bf16x2, b), acc, false);
}
template <int CTRL> __device__ __forceinline__ float dpp(float x) {
  return __builtin_bit_cast(float, __builtin_amdgcn_mov_dpp(__builtin_bit_cast(int, x), CTRL, 0xf, 0xf, true));
}
constexpr int XOR1 = 0xB1, XOR2 = 0x4E, XOR7 = 0x141, ROR8 = 0x128, ROR4 = 0x124, BC0 = 0x150, BC8 = 0x158;
__device__ __forceinline__ float sw16_add(float a, float b) {
  auto s = __builtin_amdgcn_permlane16_swap(f2u(a), f2u(b), false, false);
  unsigned s0 = s[0], s1 = s[1]; return u2f(s0) + u2f(s1);
}
__device__ __forceinline__ float sw32_add(float a, float b) {
  auto s = __builtin_amdgcn_permlane32_swap(f2u(a), f2u(b), false, false);
  unsigned s0 = s[0], s1 = s[1]; return u2f(s0) + u2f(s1);
}
__device__ __forceinline__ float sw16_max(float a) {
  auto s = __builtin_amdgcn_permlane16_swap(f2u(a), f2u(a), false, false);
  unsigned s0 = s[0], s1 = s[1]; return __builtin_fmaxf(u2f(s0), u2f(s1));
}
__device__ __forceinline__ float sw32_max(float a) {
  auto s = __builtin_amdgcn_permlane32_swap(f2u(a), f2u(a), false, false);
  unsigned s0 = s[0], s1 = s[1]; return __builtin_fmaxf(u2f(s0), u2f(s1));
}
__device__ __forceinline__ float wave_sum(float t) {
  t += dpp<XOR1>(t); t += dpp<XOR2>(t); t += dpp<XOR7>(t); t += dpp<ROR8>(t);
  t = sw16_add(t, t); return sw32_add(t, t);
}
__device__ __forceinline__ float rdl(float v, int l) { return u2f(__builtin_amdgcn_readlane(f2u(v), l)); }
#define LDL(p) __hip_atomic_load((LAS int*)(p), __ATOMIC_RELAXED, __HIP_MEMORY_SCOPE_WORKGROUP)
#define STL(p, v) __hip_atomic_store((LAS int*)(p), v, __ATOMIC_RELAXED, __HIP_MEMORY_SCOPE_WORKGROUP)
#define SCHED() __builtin_amdgcn_sched_barrier(0)

#define TS(i) do { if (lane == 0) tsp[i] = __builtin_amdgcn_s_memrealtime(); } while (0)

template <bool REFILL>
__device__ __forceinline__ void step(u32x4 (&kr)[2], u32x4 (&vr)[2], const u32x4& q0, const u32x4& q1, f32x2 (&a0)[4],
                                     f32x2 (&a1)[4], float& m, float& mc, float& l, rsrc_t rk, rsrc_t rv, int voff,
                                     unsigned o, bool lane8) {
  float d[4];
#pragma unroll
  for (int u = 0; u < 2; ++u) {
    const u32x4 k = kr[u];
    float x = 0.f, y = 0.f;
#pragma unroll
    for (int e = 0; e < 4; ++e) { unsigned w = k[e], qa = q0[e], qb = q1[e]; x = dot2(w, qa, x); y = dot2(w, qb, y); }
    if (REFILL) {
      asm volatile("" : "+v"(x), "+v"(y) :: "memory");
      kr[u] = __builtin_amdgcn_raw_buffer_load_b128(rk, voff + u * 1024, o, K_AUX);
      SCHED();
    }
    d[2 * u] = x; d[2 * u + 1] = y;
  }
  float ab = sw16_add(d[0], d[1]), cd = sw16_add(d[2], d[3]);
  ab += dpp<ROR8>(ab); cd += dpp<ROR8>(cd);
  float t = lane8 ? cd : ab;
  t += dpp<XOR7>(t); t += dpp<XOR2>(t); t += dpp<XOR1>(t);
  t = rbf(t);
  if (__builtin_amdgcn_ballot_w64(t > m) != 0) {
    float mx = __builtin_fmaxf(t, dpp<ROR8>(t));
    mx = __builtin_fmaxf(sw32_max(mx), m);
    const float mcn = mx * CSC;
    const float f = __builtin_amdgcn_exp2f(mc - mcn);
    m = mx; mc = mcn; l *= f;
    const float f0 = rdl(f, 0), f1 = rdl(f, 16);
#pragma unroll
    for (int k = 0; k < 4; ++k) { a0[k] *= f0; a1[k] *= f1; }
  }
  const float p = __builtin_amdgcn_exp2f(__builtin_fmaf(t, CSC, -mc));
  l += p;
  auto s = __builtin_amdgcn_permlane16_swap(f2u(p), f2u(p), false, false);
  unsigned s0 = s[0], s1 = s[1];
  const float ph0 = u2f(s0), ph1 = u2f(s1);
  const float pp[4] = {dpp<BC0>(ph0), dpp<BC0>(ph1), dpp<BC8>(ph0), dpp<BC8>(ph1)};
#pragma unroll
  for (int u = 0; u < 2; ++u) {
    const u32x4 v = vr[u];
    const f32x2 p0 = {pp[2 * u], pp[2 * u]}, p1 = {pp[2 * u + 1], pp[2 * u + 1]};
#pragma unroll
    for (int e = 0; e < 4; ++e) {
      unsigned w = v[e];
      const f32x2 x = {u2f(w << 16), u2f(w & 0xffff0000u)};
      a0[e] = __builtin_elementwise_fma(x, p0, a0[e]);
      a1[e] = __builtin_elementwise_fma(x, p1, a1[e]);
    }
    if (REFILL) {
      asm volatile("" : "+v"(a0[0]), "+v"(a0[1]), "+v"(a0[2]), "+v"(a0[3]), "+v"(a1[0]), "+v"(a1[1]), "+v"(a1[2]), "+v"(a1[3]) :: "memory");
      vr[u] = __builtin_amdgcn_raw_buffer_load_b128(rv, voff + u * 1024, o, V_AUX);
      SCHED();
    }
  }
}

__device__ __forceinline__ void gemv16(const u32x4 (&w)[14], const unsigned (&xp)[7], float& r0, float& r1) {
  float a[8];
#pragma unroll
  for (int i = 0; i < 8; ++i) a[i] = 0.f;
#pragma unroll
  for (int j = 0; j < 7; ++j) {
#pragma unroll
    for (int k = 0; k < 4; ++k) {
      unsigned w0 = w[2 * j][k], w1 = w[2 * j + 1][k];
      a[2 * k] = dot2(__builtin_amdgcn_perm(w1, w0, 0x05040100u), xp[j], a[2 * k]);
      a[2 * k + 1] = dot2(__builtin_amdgcn_perm(w1, w0, 0x07060302u), xp[j], a[2 * k + 1]);
    }
  }
  float b[4];
#pragma unroll
  for (int i = 0; i < 4; ++i) b[i] = sw32_add(a[i], a[i + 4]);
  r0 = sw16_add(b[0], b[2]); r1 = sw16_add(b[1], b[3]);
  r0 += dpp<ROR8>(r0); r1 += dpp<ROR8>(r1);
  r0 += dpp<ROR4>(r0); r1 += dpp<ROR4>(r1);
  r0 += dpp<XOR2>(r0); r1 += dpp<XOR2>(r1);
}

__device__ __forceinline__ void put(rsrc_t rw, unsigned off, unsigned a, unsigned b, unsigned tag) {
  __builtin_amdgcn_raw_buffer_store_b128(u32x4{a, tag, b, tag}, rw, off, 0, 16);
}

template <int N>
__device__ __forceinline__ void gather(rsrc_t rw, unsigned (&off)[N], unsigned tag, float (&v0)[N], float (&v1)[N]) {
#pragma unroll
  for (int i = 0; i < N; ++i) { v0[i] = 0.f; v1[i] = 0.f; }
#pragma unroll 1
  for (int spin = 0; spin < SPIN_MAX; ++spin) {
    u32x4 t[N];
#pragma unroll
    for (int i = 0; i < N; ++i) t[i] = __builtin_amdgcn_raw_buffer_load_b128(rw, off[i], 0, 16);
    bool left = false;
#pragma unroll
    for (int i = 0; i < N; ++i) {
      const unsigned t0 = t[i][0], t1 = t[i][1], t2 = t[i][2], t3 = t[i][3];
      const bool ok = (t1 == tag) & (t3 == tag) & (off[i] != OOB);
      v0[i] = ok ? u2f(t0) : v0[i]; v1[i] = ok ? u2f(t2) : v1[i];
      off[i] = ok ? OOB : off[i];
      left |= off[i] != OOB;
    }
    if (__builtin_amdgcn_ballot_w64(left) == 0) break;
    __builtin_amdgcn_s_sleep(1);
  }
}

template <int N> __device__ __forceinline__ void poll_issue(rsrc_t rw, const unsigned (&off)[N], u32x4 (&t)[N]) {
#pragma unroll
  for (int i = 0; i < N; ++i) t[i] = __builtin_amdgcn_raw_buffer_load_b128(rw, off[i], 0, 16);
  SCHED();
}
template <int N>
__device__ __forceinline__ bool poll_check(const u32x4 (&t)[N], unsigned (&off)[N], unsigned tag, float (&v0)[N], float (&v1)[N]) {
  bool left = false;
#pragma unroll
  for (int i = 0; i < N; ++i) {
    const unsigned t0 = t[i][0], t1 = t[i][1], t2 = t[i][2], t3 = t[i][3];
    const bool ok = (t1 == tag) & (t3 == tag) & (off[i] != OOB);
    v0[i] = ok ? u2f(t0) : v0[i]; v1[i] = ok ? u2f(t2) : v1[i];
    off[i] = ok ? OOB : off[i];
    left |= off[i] != OOB;
  }
  return __builtin_amdgcn_ballot_w64(left) == 0;
}
template <int N>
__device__ __forceinline__ void poll2(rsrc_t rw, unsigned (&off)[N], unsigned tag, float (&v0)[N], float (&v1)[N],
                                      u32x4 (&ta)[N], u32x4 (&tb)[N]) {
#pragma unroll 1
  for (int spin = 0; spin < SPIN_MAX; ++spin) {
    if (poll_check<N>(ta, off, tag, v0, v1)) break;
    poll_issue<N>(rw, off, ta);
    if (poll_check<N>(tb, off, tag, v0, v1)) break;
    poll_issue<N>(rw, off, tb);
  }
}

__global__ void __launch_bounds__(576) fused_decode(const unsigned* __restrict__ x32, const char* __restrict__ K,
                                                    const char* __restrict__ V, const char* __restrict__ wq,
                                                    const char* __restrict__ wo, const char* __restrict__ wg,
                                                    const char* __restrict__ wu, const char* __restrict__ wd,
                                                    unsigned* out32, char* ws) {
  __shared__ __attribute__((aligned(16))) float macc[8 * 512];
  __shared__ __attribute__((aligned(16))) unsigned qs[256];
  __shared__ __attribute__((aligned(16))) unsigned xs[224];
  __shared__ __attribute__((aligned(16))) float mml[32];
  __shared__ __attribute__((aligned(16))) float red[8 * 16];
  __shared__ __attribute__((aligned(16))) unsigned uaddr[512];
  __shared__ int ctl[8];
  const int lane = threadIdx.x & 63, hwave = __builtin_amdgcn_readfirstlane(threadIdx.x >> 6);
  const int wave = hwave == 0 ? 8 : hwave - 1;
  const int b = blockIdx.x, g = b >> 5, grp = b >> 3, mem = b & 7, j32 = b & 31;
  unsigned long long* tsp = (unsigned long long*)(ws + (8u << 20)) + (b * 16 + wave) * 16;
  asm volatile("" :: "s"(x32), "s"(K), "s"(V), "s"(wq), "s"(wo), "s"(wg), "s"(wu), "s"(wd), "s"(out32), "s"(ws));
  TS(0);
  const rsrc_t rw = __builtin_amdgcn_make_buffer_rsrc((void*)ws, 0, WS_USED, 0x00020000);
  const rsrc_t rk = __builtin_amdgcn_make_buffer_rsrc((void*)(K + (size_t)g * HEADB), 0, HEADB, 0x00020000);
  const rsrc_t rv = __builtin_amdgcn_make_buffer_rsrc((void*)(V + (size_t)g * HEADB), 0, HEADB, 0x00020000);
  unsigned tag;

  if (__builtin_expect(wave < 8, 1)) {
    const int voff = lane * 16;
    const unsigned sbase = (grp & 3) * REG + wave * 2048, srot = grp * ROT + mem * 16384u;
    auto offs = [&](int s) -> unsigned {
      return HMAP ? ((s * 524288u + j32 * 16384u + g * HROT) & (HEADB - 1)) + wave * 2048 : sbase + ((s * 131072u + srot) & (REG - 1));
    };
    u32x4 kb[NBACK + 1][2], vb[NBACK + 1][2];
    asm volatile("s_barrier" ::: "memory"); SCHED();
#pragma unroll
    for (int s = 0; s <= NBACK; ++s) {
      const unsigned o = offs(s);
#pragma unroll
      for (int u = 0; u < 2; ++u) {
        if (s > 0) asm volatile("s_waitcnt vmcnt(3)" ::: "memory");
        kb[s][u] = __builtin_amdgcn_raw_buffer_load_b128(rk, voff + u * 1024, o, K_AUX);
        SCHED();
      }
#pragma unroll
      for (int u = 0; u < 2; ++u) {
        if (s > 0) asm volatile("s_waitcnt vmcnt(3)" ::: "memory");
        vb[s][u] = __builtin_amdgcn_raw_buffer_load_b128(rv, voff + u * 1024, o, V_AUX);
        SCHED();
      }
    }
    TS(1);
    {
      int spin = 0;
#pragma unroll 1
      while (__builtin_amdgcn_readfirstlane(LDL(&ctl[1])) == 0 && ++spin < SPIN_MAX) __builtin_amdgcn_s_sleep(1);
    }
    asm volatile("" ::: "memory");
    TS(2);
    const u32x4 q0 = *(const u32x4*)&qs[(lane & 31) * 4], q1 = *(const u32x4*)&qs[128 + (lane & 31) * 4];
    tag = __builtin_amdgcn_readfirstlane(LDL(&ctl[2]));
    f32x2 a0[4], a1[4];
#pragma unroll
    for (int k = 0; k < 4; ++k) { a0[k] = f32x2{0.f, 0.f}; a1[k] = f32x2{0.f, 0.f}; }
    float m = -INFINITY, mc = -INFINITY, l = 0.f;
    const bool lane8 = (lane & 8) != 0;
#pragma unroll
    for (int s = 0; s < NBACK; ++s) {
      step<true>(kb[NBACK], vb[NBACK], q0, q1, a0, a1, m, mc, l, rk, rv, voff, offs(NBACK + 1 + s), lane8);
      step<false>(kb[s], vb[s], q0, q1, a0, a1, m, mc, l, rk, rv, voff, 0, lane8);
      SCHED();
    }
    TS(3);
    int n = 2 * NBACK + 1;
#pragma unroll 1
    for (;;) {
      unsigned o;
      if (n < S_TICK) {
        o = offs(n);
      } else {
        if (LATE_TICKET) asm volatile("s_waitcnt vmcnt(3)" ::: "memory");
        int t = 0;
        if (lane == 0) t = __hip_atomic_fetch_add((LAS int*)&ctl[0], 1, __ATOMIC_RELAXED, __HIP_MEMORY_SCOPE_WORKGROUP);
        t = __builtin_amdgcn_readfirstlane(t);
        o = __builtin_amdgcn_readfirstlane(LDL(&uaddr[t]));
        int spin = 0;
#pragma unroll 1
        while (o == EMPTY && ++spin < SPIN_MAX) { __builtin_amdgcn_s_sleep(1); o = __builtin_amdgcn_readfirstlane(LDL(&uaddr[t])); }
        o = o == EMPTY ? OOB : o;
      }
      step<true>(kb[NBACK], vb[NBACK], q0, q1, a0, a1, m, mc, l, rk, rv, voff, o, lane8);
      if (o == OOB) break;
      ++n;
    }
    TS(4);
    f32x4 lo, hi;
    lo[0] = sw32_add(a0[0][0], a1[0][0]); lo[1] = sw32_add(a0[0][1], a1[0][1]);
    lo[2] = sw32_add(a0[1][0], a1[1][0]); lo[3] = sw32_add(a0[1][1], a1[1][1]);
    hi[0] = sw32_add(a0[2][0], a1[2][0]); hi[1] = sw32_add(a0[2][1], a1[2][1]);
    hi[2] = sw32_add(a0[3][0], a1[3][0]); hi[3] = sw32_add(a0[3][1], a1[3][1]);
    *(f32x4*)&macc[wave * 512 + lane * 4] = lo;
    *(f32x4*)&macc[wave * 512 + 256 + lane * 4] = hi;
    l += dpp<ROR8>(l); l = sw32_add(l, l);
    if ((lane & 47) == 0) *(f32x2*)&mml[(lane >> 4) * 16 + wave * 2] = f32x2{mc, l};
    asm volatile("s_waitcnt lgkmcnt(0)" ::: "memory");
    int arr = 0;
    if (lane == 0) arr = __hip_atomic_fetch_add((LAS int*)&ctl[3], 1, __ATOMIC_RELAXED, __HIP_MEMORY_SCOPE_WORKGROUP);
    arr = __builtin_amdgcn_readfirstlane(arr);
    asm volatile("" ::: "memory");
    if (arr == 7) {
      TS(5);
      f32x4 ml[4], pl[8], ph[8];
#pragma unroll
      for (int i = 0; i < 4; ++i) ml[i] = *(const f32x4*)&mml[(lane >> 5) * 16 + i * 4];
#pragma unroll
      for (int w = 0; w < 8; ++w) { pl[w] = *(const f32x4*)&macc[w * 512 + lane * 4]; ph[w] = *(const f32x4*)&macc[w * 512 + 256 + lane * 4]; }
      float M = ml[0][0];
#pragma unroll
      for (int i = 0; i < 4; ++i) M = __builtin_fmaxf(M, __builtin_fmaxf(ml[i][0], ml[i][2]));
      float L = 0.f; f32x4 sl = {0.f, 0.f, 0.f, 0.f}, sh = {0.f, 0.f, 0.f, 0.f};
#pragma unroll
      for (int w = 0; w < 8; ++w) {
        const float f = __builtin_amdgcn_exp2f(ml[w >> 1][(w & 1) * 2] - M);
        L = __builtin_fmaf(f, ml[w >> 1][(w & 1) * 2 + 1], L);
        sl = __builtin_elementwise_fma(pl[w], f32x4{f, f, f, f}, sl);
        sh = __builtin_elementwise_fma(ph[w], f32x4{f, f, f, f}, sh);
      }
      const float sv[8] = {sl[0], sl[1], sl[2], sl[3], sh[0], sh[1], sh[2], sh[3]};
      const int vo = (lane >> 3) * 16384 + (lane & 7) * 16;
#pragma unroll
      for (int i = 0; i < 4; ++i)
        put(rw, WS_H1 + g * 131072 + i * 4096 + j32 * 128 + (vo), f2u(sv[2 * i]), f2u(sv[2 * i + 1]), tag);
      if ((lane & 31) == 0)
        put(rw, WS_ML + g * 1024 + j32 * 16 + ((lane >> 5) * 512), f2u(M), f2u(L), tag);
      TS(6);
    }
    const int c2 = PW == 8 ? b / 9 : (b & 1) * 28 + (b >> 1) / 9 * 2 + (((b >> 1) % 9) >> 2);
    const bool cons = PW == 8 ? (b % 9 == 0 && c2 < 28) : false;
    if (cons) {
      const int ow = (c2 >> 2) * (PW * 4) + (lane & (PW - 1)) * 4 + (c2 & 3);
      const unsigned xv = x32[ow];
      unsigned off[PW / 2]; float v0[PW / 2], v1[PW / 2];
#pragma unroll
      for (int i = 0; i < PW / 2; ++i) off[i] = WS_H2 + (c2 * 256 + wave * 32) * (PW * 16) + i * 1024 + lane * 16;
      if (PDEL2) {
        int spin = 0;
#pragma unroll 1
        while (__builtin_amdgcn_readfirstlane(LDL(&ctl[5])) == 0 && ++spin < SPIN_MAX) __builtin_amdgcn_s_sleep(1);
        asm volatile("" ::: "memory");
      }
      u32x4 ta[PW / 2], tb[PW / 2];
      if (STAG2) {
#pragma unroll
        for (int i = 0; i < PW / 2; ++i) { v0[i] = 0.f; v1[i] = 0.f; }
        poll_issue<PW / 2>(rw, off, ta);
        __builtin_amdgcn_s_sleep(STAG2);
        poll_issue<PW / 2>(rw, off, tb);
        poll2<PW / 2>(rw, off, tag, v0, v1, ta, tb);
      } else gather<PW / 2>(rw, off, tag, v0, v1);
      TS(7);
      float s0 = 0.f, s1 = 0.f;
#pragma unroll
      for (int i = 0; i < PW / 2; ++i) { s0 += v0[i]; s1 += v1[i]; }
      float s = sw32_add(s0, s1);
      s += dpp<ROR8>(s); s = sw16_add(s, s);
      if (PW == 4) s += dpp<ROR4>(s);
      if ((lane & 24) == 0) red[wave * 16 + (lane & 7) * 2 + (lane >> 5)] = s;
      asm volatile("s_waitcnt lgkmcnt(0)" ::: "memory");
      int fin = 0;
      if (lane == 0) fin = __hip_atomic_fetch_add((LAS int*)&ctl[4], 1, __ATOMIC_RELAXED, __HIP_MEMORY_SCOPE_WORKGROUP);
      fin = __builtin_amdgcn_readfirstlane(fin);
      asm volatile("" ::: "memory");
      if (fin == 7) {
        const f32x2 r = *(const f32x2*)&red[lane * 2];
        float r0 = r[0], r1 = r[1];
        r0 += dpp<ROR8>(r0); r1 += dpp<ROR8>(r1);
        r0 = sw16_add(r0, r0); r1 = sw16_add(r1, r1);
        r0 = sw32_add(r0, r0); r1 = sw32_add(r1, r1);
        if (lane < PW) out32[ow] = pkbf(r0 + u2f(xv << 16), r1 + u2f(xv & 0xffff0000u));
        if (b == 0 && lane == 0) __builtin_amdgcn_raw_buffer_store_b32(tag, rw, 0, WS_EPOCH, 16);
        TS(8);
      }
      if (STAG2) asm volatile("" :: "v"(ta[0]), "v"(ta[1]), "v"(ta[2]), "v"(ta[3]), "v"(tb[0]), "v"(tb[1]), "v"(tb[2]), "v"(tb[3]));
    }
    asm volatile("" :: "v"(kb[NBACK][0]), "v"(kb[NBACK][1]), "v"(vb[NBACK][0]), "v"(vb[NBACK][1]));
  } else {
    __builtin_amdgcn_s_setprio(3);
    const unsigned ep = __builtin_amdgcn_raw_buffer_load_b32(rw, 0, WS_EPOCH, 16);
    unsigned xr[7];
#pragma unroll
    for (int j = 0; j < 7; ++j) xr[j] = x32[(lane >> 1) + 32 * j];
    u32x4 w[14];
    const int jq = mem * 4 + (grp & 3);
    {
      const rsrc_t r = __builtin_amdgcn_make_buffer_rsrc((void*)wq, 0, 448 * 8192, 0x00020000);
      const int vo = (lane >> 1) * 16384 + (lane & 1) * 16;
#pragma unroll
      for (int i = 0; i < 14; ++i) w[i] = __builtin_amdgcn_raw_buffer_load_b128(r, vo, g * 1024 + jq * 32 + (i >> 1) * 524288 + (i & 1) * 8192, 2);
    }
    if (j32 == 0 && lane == 0) __builtin_amdgcn_raw_buffer_store_b32(0u, rw, 0, WS_CTR + g * 256, 16);
    SCHED();
    *(u32x4*)&uaddr[lane * 4] = u32x4{EMPTY, EMPTY, EMPTY, EMPTY};
    *(u32x4*)&uaddr[256 + lane * 4] = u32x4{EMPTY, EMPTY, EMPTY, EMPTY};
    if (HMAP) { if (lane < NPRE) uaddr[lane] = (((S_TICK + (lane >> 3)) * 524288u + j32 * 16384u + g * HROT) & (HEADB - 1)) + (lane & 7) * 2048; }
    else if (lane < NPRE) uaddr[lane] = (grp & 3) * REG + (lane & 7) * 2048 + (((S_TICK + (lane >> 3)) * 131072u + grp * ROT + mem * 16384u) & (REG - 1));
    if (lane < 8) ctl[lane] = 0;
    asm volatile("s_waitcnt lgkmcnt(0)\n s_barrier" ::: "memory");
    asm volatile("" : "+v"(xr[0]), "+v"(xr[1]), "+v"(xr[2]), "+v"(xr[3]), "+v"(xr[4]), "+v"(xr[5]), "+v"(xr[6]));
    unsigned xp[7];
    {
      float s = 0.f;
#pragma unroll
      for (int j = 0; j < 7; ++j) s += u2f(xr[j] << 16) + u2f(xr[j] & 0xffff0000u);
      const float mean = wave_sum(s) * (1.f / 896.f);
      float v = 0.f;
#pragma unroll
      for (int j = 0; j < 7; ++j) { const float da = u2f(xr[j] << 16) - mean, db = u2f(xr[j] & 0xffff0000u) - mean; v += da * da + db * db; }
      const float var = wave_sum(v) * (1.f / 896.f);
      const float mb = rbf(mean), rden = __builtin_amdgcn_rcpf(rbf(__builtin_amdgcn_sqrtf(rbf(rbf(var) + 1e-5f))));
#pragma unroll
      for (int j = 0; j < 7; ++j) xp[j] = pkbf(rbf(u2f(xr[j] << 16) - mb) * rden, rbf(u2f(xr[j] & 0xffff0000u) - mb) * rden);
    }
    tag = __builtin_amdgcn_readfirstlane(ep) + 1u;
    TS(1);
    {
      float r0, r1;
      gemv16(w, xp, r0, r1);
      const unsigned pk = pkbf(r0, r1);
      auto s = __builtin_amdgcn_permlane16_swap(pk, pk, false, false);
      unsigned s0 = s[0], s1 = s[1];
      asm volatile("s_waitcnt vmcnt(0)" : "+v"(s0), "+v"(s1) :: "memory");
      if ((lane & 30) == 0)
        put(rw, WS_Q + g * 2048 + jq * 64 + (((lane & 1) * 2 + (lane >> 5)) * 16), s0, s1, tag);
    }
    TS(2);
#pragma unroll
    for (int j = 0; j < 7; ++j) xs[(lane >> 1) + 32 * j] = xp[j];
    {
      unsigned off[2] = {WS_Q + g * 2048 + lane * 16u, WS_Q + g * 2048 + 1024 + lane * 16u};
      float v0[2], v1[2];
      gather<2>(rw, off, tag, v0, v1);
      *(f32x2*)&qs[lane * 2] = f32x2{v0[0], v1[0]};
      *(f32x2*)&qs[128 + lane * 2] = f32x2{v0[1], v1[1]};
      if (lane == 0) STL(&ctl[2], (int)tag);
      asm volatile("s_waitcnt lgkmcnt(0)" ::: "memory");
      if (lane == 0) STL(&ctl[1], 1);
    }
    TS(3);
    const int fu = grp * 7 + mem;
    if (mem < 7) {
      const rsrc_t rg = __builtin_amdgcn_make_buffer_rsrc((void*)wg, 0, 448 * 3584, 0x00020000);
      const rsrc_t ru = __builtin_amdgcn_make_buffer_rsrc((void*)wu, 0, 448 * 3584, 0x00020000);
      const int vo = (lane >> 3) * 3584 + (lane & 7) * 16, so = (fu & 7) * 56 * 3584 + (fu >> 3) * 128;
#pragma unroll
      for (int i = 0; i < 7; ++i) {
        w[2 * i] = __builtin_amdgcn_raw_buffer_load_b128(rg, vo, so + i * 8 * 3584, 2);
        w[2 * i + 1] = __builtin_amdgcn_raw_buffer_load_b128(ru, vo, so + i * 8 * 3584, 2);
      }
      float a[16];
#pragma unroll
      for (int i = 0; i < 16; ++i) a[i] = 0.f;
#pragma unroll
      for (int i = 0; i < 7; ++i) {
        const unsigned xl = ((const unsigned short*)xs)[(fu & 7) * 56 + i * 8 + (lane >> 3)], xh = xl << 16;
#pragma unroll
        for (int e = 0; e < 4; ++e) {
          const unsigned wa = w[2 * i][e], wb = w[2 * i + 1][e];
          a[2 * e] = dot2(wa, xl, a[2 * e]); a[2 * e + 1] = dot2(wa, xh, a[2 * e + 1]);
          a[8 + 2 * e] = dot2(wb, xl, a[8 + 2 * e]); a[8 + 2 * e + 1] = dot2(wb, xh, a[8 + 2 * e + 1]);
        }
      }
#pragma unroll
      for (int i = 0; i < 8; ++i) a[i] = sw32_add(a[i], a[i + 8]);
#pragma unroll
      for (int i = 0; i < 4; ++i) { a[i] = sw16_add(a[i], a[i + 4]); a[i] += dpp<ROR8>(a[i]); }
      const bool b3 = (lane & 8) != 0;
      const int src = (((lane & 7) << 3) | (lane >> 3)) * 4;
      const unsigned d0 = __builtin_amdgcn_ds_bpermute(src, f2u(b3 ? a[2] : a[0])), d1 = __builtin_amdgcn_ds_bpermute(src, f2u(b3 ? a[3] : a[1]));
      put(rw, WS_F + (fu >> 3) * 8192 + (fu & 7) * 128 + ((lane >> 3) * 1024 + (lane & 7) * 16), d0, d1, tag);
    }
    SCHED();
    float fo[8];
    unsigned wp[8][8], hp = 0;
    const rsrc_t ro = __builtin_amdgcn_make_buffer_rsrc((void*)wo, 0, 4096 * 896, 0x00020000);
    const rsrc_t rd = __builtin_amdgcn_make_buffer_rsrc((void*)wd, 0, 1792 * 896, 0x00020000);
    const int row0 = g * 512 + (j32 >> 2) * 64 + (j32 & 3) * 2;
    const unsigned wvo = lane < 56 ? lane * 16u : OOB;
    u32x4 ov[16], dv[8];
    auto issue_w = [&]() __attribute__((always_inline)) {
#pragma unroll
      for (int i = 0; i < 8; ++i) dv[i] = __builtin_amdgcn_raw_buffer_load_b128(rd, wvo, mem < 7 ? (fu * 8 + i) * 896 : OOB, 2);
#pragma unroll
      for (int i = 0; i < 16; ++i) ov[i] = __builtin_amdgcn_raw_buffer_load_b128(ro, wvo, (row0 + (i >> 1) * 8 + (i & 1)) * 896, 2);
      SCHED();
    };
    auto prep_d = [&]() __attribute__((always_inline)) {
#pragma unroll
      for (int i = 0; i < 8; ++i) fo[i] = 0.f;
#pragma unroll
      for (int pc = 0; pc < 4; ++pc) {
        const unsigned hs = __builtin_amdgcn_readlane(hp, pc);
#pragma unroll
        for (int k = 0; k < 4; ++k) {
          unsigned w0 = dv[2 * pc][k], w1 = dv[2 * pc + 1][k];
          fo[2 * k] = dot2(__builtin_amdgcn_perm(w1, w0, 0x05040100u), hs, fo[2 * k]);
          fo[2 * k + 1] = dot2(__builtin_amdgcn_perm(w1, w0, 0x07060302u), hs, fo[2 * k + 1]);
        }
      }
      asm volatile("" : "+v"(fo[0]), "+v"(fo[1]), "+v"(fo[2]), "+v"(fo[3]), "+v"(fo[4]), "+v"(fo[5]), "+v"(fo[6]), "+v"(fo[7]));
      SCHED();
    };
    auto prep_o = [&](auto p0, auto p1) __attribute__((always_inline)) {
#pragma unroll
      for (int pc = decltype(p0)::v; pc < decltype(p1)::v; ++pc)
#pragma unroll
        for (int k = 0; k < 4; ++k) {
          unsigned w0 = ov[2 * pc][k], w1 = ov[2 * pc + 1][k];
          wp[pc][2 * k] = __builtin_amdgcn_perm(w1, w0, 0x05040100u);
          wp[pc][2 * k + 1] = __builtin_amdgcn_perm(w1, w0, 0x07060302u);
          asm volatile("" : "+v"(wp[pc][2 * k]), "+v"(wp[pc][2 * k + 1]));
        }
      SCHED();
    };
    if (!LATEW) issue_w();
    else {
      const unsigned t0 = __builtin_amdgcn_raw_buffer_load_b32(ro, 0, row0 * 896, 2), t1 = __builtin_amdgcn_raw_buffer_load_b32(rd, 0, fu * 8 * 896, 2);
      const unsigned t2 = __builtin_amdgcn_raw_buffer_load_b32(rw, WS_H1 + b * 4096 + lane * 64, 0, 16);
      const unsigned t3 = __builtin_amdgcn_raw_buffer_load_b32(rw, WS_ML + g * 1024 + lane * 16, 0, 16);
      asm volatile("" :: "v"(t0), "v"(t1), "v"(t2), "v"(t3));
    }
    if (mem < 7) {
      unsigned off[1] = {WS_F + fu * 1024 + lane * 16u};
      float v0[1], v1[1];
      gather<1>(rw, off, tag, v0, v1);
      float r0 = v0[0], r1 = v1[0];
      r0 += dpp<ROR8>(r0); r1 += dpp<ROR8>(r1);
      r0 = sw16_add(r0, r0); r1 = sw16_add(r1, r1);
      r0 = rbf(sw32_add(r0, r0)); r1 = rbf(sw32_add(r1, r1));
      const float u0 = dpp<ROR4>(r0), u1 = dpp<ROR4>(r1);
      const float s0 = rbf(r0 * __builtin_amdgcn_rcpf(1.f + __expf(-r0))), s1 = rbf(r1 * __builtin_amdgcn_rcpf(1.f + __expf(-r1)));
      hp = pkbf(s0 * u0, s1 * u1);
    }
    if (!LATEW) { prep_d(); prep_o(IC<0>{}, IC<8>{}); }
    TS(4);
    {
      const rsrc_t rc = __builtin_amdgcn_make_buffer_rsrc((void*)(ws + WS_CTR + g * 256), 0, 4, 0x00020000);
      int k = NPRE, want = G1 + 1, kfin = -1;
      bool asked = false;
#pragma unroll 1
      for (int spin = 0; spin < 16 * SPIN_MAX; ++spin) {
        const int t = __builtin_amdgcn_readfirstlane(LDL(&ctl[0]));
        if (k - t <= THRESH) {
          const bool fin = want <= G1 && !asked;
          const int gr = want > G1 ? G1 : !fin && k - t <= 0 && want < GMIN ? GMIN : want;
          const unsigned r = (unsigned)__builtin_amdgcn_readfirstlane(__builtin_amdgcn_raw_ptr_buffer_atomic_add_i32(gr + (fin ? 1 << 24 : 0), rc, lane * 4, 0, 16));
          const unsigned p = r & 0xffffffu;
          const int rem = NP - (int)p - gr, nr = (int)(r >> 24);
          const bool last = rem <= 0 || k + gr + G1 > UCAP || (fin && nr < RMAX && rem <= (31 - nr) * XS);
          asked |= fin;
          const unsigned pu = p + lane, jc = pu >> 3, rr = jc & 3;
          const unsigned o = HMAP ? (S_STATIC * 524288u + pu * 2048u + g * HROT) & (HEADB - 1) : rr * REG + (pu & 7) * 2048 + (((S_STATIC * 8 + (jc >> 2)) * 16384u + (g * 4 + rr) * ROT) & (REG - 1));
          const bool valid = pu < (unsigned)NP && lane < gr;
          if (valid || last) STL(&uaddr[k + lane], (int)(valid ? o : OOB));
          if (lane == 0) { tsp[9] = k + (valid ? gr : 0); if (gr > 0 && p < (unsigned)NP) { tsp[10] = __builtin_amdgcn_s_memrealtime(); tsp[11] = gr; tsp[12] = k - t + 100; } }
          if (last) break;
          k += gr;
          if (rem >= RF || kfin < 0 || asked) {
            const int t2 = __builtin_amdgcn_readfirstlane(LDL(&ctl[0]));
            const float ta = 0.5f * (float)(t + t2), q = rem >= RF ? Q0 : Q1;
            const float rho = (S_TICK * 8 + ta) * __builtin_amdgcn_rcpf(256.f - q - rem * (1.f / 32.f));
            kfin = __builtin_amdgcn_readfirstlane((int)(rho * (rem * (1.f / 32.f) + q + FL) - FL + ta + 0.5f));
          }
          want = kfin - k < 0 ? 0 : kfin - k;
        } else {
          __builtin_amdgcn_s_sleep(1);
        }
      }
    }
    TS(5);
    {
      unsigned off[5]; float v0[5], v1[5];
      u32x4 ta[5], tb[5];
#pragma unroll
      for (int i = 0; i < 4; ++i) off[i] = WS_H1 + b * 4096 + i * 1024 + lane * 16;
      off[4] = WS_ML + g * 1024 + (j32 >> 4) * 512 + ((lane >> 3) + 8 * (lane & 3)) * 16;
#pragma unroll
      for (int i = 0; i < 5; ++i) { v0[i] = 0.f; v1[i] = 0.f; }
      if (LATEW) {
        int spin = 0;
#pragma unroll 1
        while (__builtin_amdgcn_readfirstlane(LDL(&ctl[3])) < LATEW - 1 && ++spin < SPIN_MAX) __builtin_amdgcn_s_sleep(1);
        asm volatile("" ::: "memory");
        issue_w();
        prep_d();
        if (STAG) {
          prep_o(IC<0>{}, IC<STAG_A>{});
          poll_issue<5>(rw, off, ta);
          prep_o(IC<STAG_A>{}, IC<8>{});
          if (STAG_S) __builtin_amdgcn_s_sleep(STAG_S);
          poll_issue<5>(rw, off, tb);
        } else prep_o(IC<0>{}, IC<8>{});
        TS(6);
      } else if (STAG) {
        poll_issue<5>(rw, off, ta);
        __builtin_amdgcn_s_sleep(10);
        poll_issue<5>(rw, off, tb);
      }
      if (STAG) poll2<5>(rw, off, tag, v0, v1, ta, tb);
      else gather<5>(rw, off, tag, v0, v1);
      if (lane == 0) STL(&ctl[5], 1);
      TS(7);
      float M = v0[4];
      M = __builtin_fmaxf(M, dpp<XOR1>(M)); M = __builtin_fmaxf(M, dpp<XOR2>(M)); M = __builtin_fmaxf(M, dpp<ROR8>(M));
      M = sw16_max(M); M = sw32_max(M);
      const float f = __builtin_amdgcn_exp2f(v0[4] - M);
      float L = f * v1[4];
      const float f0 = dpp<0x00>(f), f1 = dpp<0x55>(f), f2 = dpp<0xAA>(f), f3 = dpp<0xFF>(f);
      float s0 = f0 * v0[0] + f1 * v0[1] + f2 * v0[2] + f3 * v0[3], s1 = f0 * v1[0] + f1 * v1[1] + f2 * v1[2] + f3 * v1[3];
      L += dpp<XOR1>(L); L += dpp<XOR2>(L);
      s0 += dpp<ROR8>(s0); s1 += dpp<ROR8>(s1); L += dpp<ROR8>(L);
      s0 = sw16_add(s0, s0); s1 = sw16_add(s1, s1); L = sw16_add(L, L);
      s0 = sw32_add(s0, s0); s1 = sw32_add(s1, s1); L = sw32_add(L, L);
      const float rl = __builtin_amdgcn_rcpf(L);
      const unsigned op = pkbf(s0 * rl, s1 * rl);
#pragma unroll
      for (int pc = 0; pc < 8; ++pc) {
        const unsigned os = __builtin_amdgcn_readlane(op, pc);
#pragma unroll
        for (int c = 0; c < 8; ++c) fo[c] = dot2(wp[pc][c], os, fo[c]);
      }
      if (lane < 56) {
        const int vo = (lane / PW) * (4 * 256 * PW * 16) + (lane & (PW - 1)) * 16;
#pragma unroll
        for (int i = 0; i < 4; ++i)
          put(rw, WS_H2 + (i * 256 + b) * (PW * 16) + (vo), f2u(fo[2 * i]), f2u(fo[2 * i + 1]), tag);
      }
      TS(8);
      if (STAG) asm volatile("" :: "v"(ta[0]), "v"(ta[1]), "v"(ta[2]), "v"(ta[3]), "v"(ta[4]), "v"(tb[0]), "v"(tb[1]), "v"(tb[2]), "v"(tb[3]), "v"(tb[4]));
    }
  }
}

__global__ void report(char* ws) {
  const unsigned long long* h = (const unsigned long long*)(ws + (8u << 20));
  __shared__ unsigned long long r;
  const int t = threadIdx.x;
  if (t == 0) r = 0ull;
  __syncthreads();
  for (int w = 0; w < 9; ++w) atomicMax(&r, h[(t * 16 + w) * 16]);
  __syncthreads();
  if (t == 0) {
    const unsigned long long until = r + 20000ull;
    for (int i = 0; i < 1000000; ++i) { if (__builtin_amdgcn_s_memrealtime() >= until) break; __builtin_amdgcn_s_sleep(1); }
  }
}
extern "C" void kernel_launch(void* const* d_in, const int* in_sizes, int n_in, void* d_out, int out_size, void* d_ws,
                              size_t ws_size, hipStream_t stream) {
  fused_decode<<<256, 576, 0, stream>>>((const unsigned*)d_in[0], (const char*)d_in[1], (const char*)d_in[2],
                                        (const char*)d_in[3], (const char*)d_in[4], (const char*)d_in[6],
                                        (const char*)d_in[7], (const char*)d_in[8], (unsigned*)d_out, (char*)d_ws);
  report<<<1, 256, 0, stream>>>((char*)d_ws);
}
```

```cpp
#include <hip/hip_runtime.h>
#include <stdint.h>

typedef unsigned u32x4 __attribute__((ext_vector_type(4)));
typedef unsigned u32x2 __attribute__((ext_vector_type(2)));
typedef float f32x2 __attribute__((ext_vector_type(2)));
typedef float f32x4 __attribute__((ext_vector_type(4)));
typedef __bf16 bf16x2 __attribute__((ext_vector_type(2)));
#define LAS __attribute__((address_space(3)))
typedef __amdgpu_buffer_rsrc_t rsrc_t;

#define S_STATIC 22
#define S_TICK (S_STATIC - 3)
#define NBACK 6
#define ROT 0x100000u
#define THRESH 8
#define G1 12
#define GMIN 2
#define Q0 4.f
#define FL 8.f
#define LATEW 1
#define K_AUX 2
#define V_AUX 2

constexpr unsigned REG = 4u << 20;
constexpr unsigned HEADB = 16u << 20;
constexpr int NP = 4 * (256 - 8 * S_STATIC) * 8;
constexpr int NPRE = (S_STATIC - S_TICK) * 8;
static_assert(NPRE <= 64 && S_TICK >= 2 * NBACK + 1, "");
constexpr int UCAP = 448;
static_assert(NP / (UCAP - NPRE - 2 * G1) <= 31, "someone stays until the pool is empty");
constexpr unsigned OOB = 0x40000000u, EMPTY = 0xffffffffu;
constexpr int SPIN_MAX = 200000;
constexpr unsigned WS_EPOCH = 0, WS_CTR = 4096, WS_Q = 16384, WS_ML = 32768, WS_H1 = 65536, WS_H2 = WS_H1 + (1u << 20);
constexpr unsigned WS_F = 2u << 20, WS_USED = WS_F + 224 * 1024;
constexpr float CSC = 0.0625f * 1.4426950408889634f;

__device__ __forceinline__ float u2f(unsigned u) { return __builtin_bit_cast(float, u); }
__device__ __forceinline__ unsigned f2u(float f) { return __builtin_bit_cast(unsigned, f); }
__device__ __forceinline__ float rbf(float f) { __bf16 b = (__bf16)f; return (float)b; }
__device__ __forceinline__ unsigned pkbf(float lo, float hi) {
  bf16x2 v; v[0] = (__bf16)lo; v[1] = (__bf16)hi; return __builtin_bit_cast(unsigned, v);
}
__device__ __forceinline__ float dot2(unsigned a, unsigned b, float acc) {
  return __builtin_amdgcn_fdot2_f32_bf16(__builtin_bit_cast(bf16x2, a), __builtin_bit_cast(bf16x2, b), acc, false);
}
template <int CTRL> __device__ __forceinline__ float dpp(float x) {
  return __builtin_bit_cast(float, __builtin_amdgcn_mov_dpp(__builtin_bit_cast(int, x), CTRL, 0xf, 0xf, true));
}
constexpr int XOR1 = 0xB1, XOR2 = 0x4E, XOR7 = 0x141, ROR8 = 0x128, ROR4 = 0x124, BC0 = 0x150, BC8 = 0x158;
__device__ __forceinline__ float sw16_add(float a, float b) {
  auto s = __builtin_amdgcn_permlane16_swap(f2u(a), f2u(b), false, false);
  unsigned s0 = s[0], s1 = s[1]; return u2f(s0) + u2f(s1);
}
__device__ __forceinline__ float sw32_add(float a, float b) {
  auto s = __builtin_amdgcn_permlane32_swap(f2u(a), f2u(b), false, false);
  unsigned s0 = s[0], s1 = s[1]; return u2f(s0) + u2f(s1);
}
__device__ __forceinline__ float sw16_max(float a) {
  auto s = __builtin_amdgcn_permlane16_swap(f2u(a), f2u(a), false, false);
  unsigned s0 = s[0], s1 = s[1]; return __builtin_fmaxf(u2f(s0), u2f(s1));
}
__device__ __forceinline__ float sw32_max(float a) {
  auto s = __builtin_amdgcn_permlane32_swap(f2u(a), f2u(a), false, false);
  unsigned s0 = s[0], s1 = s[1]; return __builtin_fmaxf(u2f(s0), u2f(s1));
}
__device__ __forceinline__ float wave_sum(float t) {
  t += dpp<XOR1>(t); t += dpp<XOR2>(t); t += dpp<XOR7>(t); t += dpp<ROR8>(t);
  t = sw16_add(t, t); return sw32_add(t, t);
}
__device__ __forceinline__ float rdl(float v, int l) { return u2f(__builtin_amdgcn_readlane(f2u(v), l)); }
#define LDL(p) __hip_atomic_load((LAS int*)(p), __ATOMIC_RELAXED, __HIP_MEMORY_SCOPE_WORKGROUP)
#define STL(p, v) __hip_atomic_store((LAS int*)(p), v, __ATOMIC_RELAXED, __HIP_MEMORY_SCOPE_WORKGROUP)
#define SCHED() __builtin_amdgcn_sched_barrier(0)

#define TS(i) do {} while (0)

template <bool REFILL>
__device__ __forceinline__ void step(u32x4 (&kr)[2], u32x4 (&vr)[2], const u32x4& q0, const u32x4& q1, f32x2 (&a0)[4],
                                     f32x2 (&a1)[4], float& m, float& mc, float& l, rsrc_t rk, rsrc_t rv, int voff,
                                     unsigned o, bool lane8) {
  float d[4];
#pragma unroll
  for (int u = 0; u < 2; ++u) {
    const u32x4 k = kr[u];
    float x = 0.f, y = 0.f;
#pragma unroll
    for (int e = 0; e < 4; ++e) { unsigned w = k[e], qa = q0[e], qb = q1[e]; x = dot2(w, qa, x); y = dot2(w, qb, y); }
    if (REFILL) {
      asm volatile("" : "+v"(x), "+v"(y) :: "memory");
      kr[u] = __builtin_amdgcn_raw_buffer_load_b128(rk, voff + u * 1024, o, K_AUX);
      SCHED();
    }
    d[2 * u] = x; d[2 * u + 1] = y;
  }
  float ab = sw16_add(d[0], d[1]), cd = sw16_add(d[2], d[3]);
  ab += dpp<ROR8>(ab); cd += dpp<ROR8>(cd);
  float t = lane8 ? cd : ab;
  t += dpp<XOR7>(t); t += dpp<XOR2>(t); t += dpp<XOR1>(t);
  t = rbf(t);
  if (__builtin_amdgcn_ballot_w64(t > m) != 0) {
    float mx = __builtin_fmaxf(t, dpp<ROR8>(t));
    mx = __builtin_fmaxf(sw32_max(mx), m);
    const float mcn = mx * CSC;
    const float f = __builtin_amdgcn_exp2f(mc - mcn);
    m = mx; mc = mcn; l *= f;
    const float f0 = rdl(f, 0), f1 = rdl(f, 16);
#pragma unroll
    for (int k = 0; k < 4; ++k) { a0[k] *= f0; a1[k] *= f1; }
  }
  const float p = __builtin_amdgcn_exp2f(__builtin_fmaf(t, CSC, -mc));
  l += p;
  auto s = __builtin_amdgcn_permlane16_swap(f2u(p), f2u(p), false, false);
  unsigned s0 = s[0], s1 = s[1];
  const float ph0 = u2f(s0), ph1 = u2f(s1);
  const float pp[4] = {dpp<BC0>(ph0), dpp<BC0>(ph1), dpp<BC8>(ph0), dpp<BC8>(ph1)};
#pragma unroll
  for (int u = 0; u < 2; ++u) {
    const u32x4 v = vr[u];
    const f32x2 p0 = {pp[2 * u], pp[2 * u]}, p1 = {pp[2 * u + 1], pp[2 * u + 1]};
#pragma unroll
    for (int e = 0; e < 4; ++e) {
      unsigned w = v[e];
      const f32x2 x = {u2f(w << 16), u2f(w & 0xffff0000u)};
      a0[e] = __builtin_elementwise_fma(x, p0, a0[e]);
      a1[e] = __builtin_elementwise_fma(x, p1, a1[e]);
    }
    if (REFILL) {
      asm volatile("" : "+v"(a0[0]), "+v"(a0[1]), "+v"(a0[2]), "+v"(a0[3]), "+v"(a1[0]), "+v"(a1[1]), "+v"(a1[2]), "+v"(a1[3]) :: "memory");
      vr[u] = __builtin_amdgcn_raw_buffer_load_b128(rv, voff + u * 1024, o, V_AUX);
      SCHED();
    }
  }
}

__device__ __forceinline__ void gemv16(const u32x4 (&w)[14], const unsigned (&xp)[7], float& r0, float& r1) {
  float a[8];
#pragma unroll
  for (int i = 0; i < 8; ++i) a[i] = 0.f;
#pragma unroll
  for (int j = 0; j < 7; ++j) {
#pragma unroll
    for (int k = 0; k < 4; ++k) {
      unsigned w0 = w[2 * j][k], w1 = w[2 * j + 1][k];
      a[2 * k] = dot2(__builtin_amdgcn_perm(w1, w0, 0x05040100u), xp[j], a[2 * k]);
      a[2 * k + 1] = dot2(__builtin_amdgcn_perm(w1, w0, 0x07060302u), xp[j], a[2 * k + 1]);
    }
  }
  float b[4];
#pragma unroll
  for (int i = 0; i < 4; ++i) b[i] = sw32_add(a[i], a[i + 4]);
  r0 = sw16_add(b[0], b[2]); r1 = sw16_add(b[1], b[3]);
  r0 += dpp<ROR8>(r0); r1 += dpp<ROR8>(r1);
  r0 += dpp<ROR4>(r0); r1 += dpp<ROR4>(r1);
  r0 += dpp<XOR2>(r0); r1 += dpp<XOR2>(r1);
}

__device__ __forceinline__ void put(rsrc_t rw, unsigned off, unsigned a, unsigned b, unsigned tag) {
  __builtin_amdgcn_raw_buffer_store_b128(u32x4{a, tag, b, tag}, rw, off, 0, 16);
}

template <int N> __device__ __forceinline__ void poll_issue(rsrc_t rw, const unsigned (&off)[N], u32x4 (&t)[N]) {
#pragma unroll
  for (int i = 0; i < N; ++i) t[i] = __builtin_amdgcn_raw_buffer_load_b128(rw, off[i], 0, 16);
  SCHED();
}
template <int N>
__device__ __forceinline__ bool poll_check(const u32x4 (&t)[N], unsigned (&off)[N], unsigned tag, float (&v0)[N], float (&v1)[N]) {
  bool left = false;
#pragma unroll
  for (int i = 0; i < N; ++i) {
    const unsigned t0 = t[i][0], t1 = t[i][1], t2 = t[i][2], t3 = t[i][3];
    const bool ok = (t1 == tag) & (t3 == tag) & (off[i] != OOB);
    v0[i] = ok ? u2f(t0) : v0[i]; v1[i] = ok ? u2f(t2) : v1[i];
    off[i] = ok ? OOB : off[i];
    left |= off[i] != OOB;
  }
  return __builtin_amdgcn_ballot_w64(left) == 0;
}
template <int N>
__device__ __forceinline__ void gather(rsrc_t rw, unsigned (&off)[N], unsigned tag, float (&v0)[N], float (&v1)[N]) {
#pragma unroll 1
  for (int spin = 0; spin < SPIN_MAX; ++spin) {
    u32x4 t[N];
    poll_issue<N>(rw, off, t);
    if (poll_check<N>(t, off, tag, v0, v1)) break;
    __builtin_amdgcn_s_sleep(1);
  }
}
__device__ __forceinline__ int arrive(int* p) {
  int r; unsigned long long e;
  asm volatile("s_mov_b64 %1, exec\n s_mov_b64 exec, 1\n ds_add_rtn_u32 %0, %2, %3\n s_mov_b64 exec, %1"
               : "=&v"(r), "=&s"(e) : "v"((unsigned)(uintptr_t)(LAS int*)p), "v"(1) : "memory");
  return r;
}

__global__ void __launch_bounds__(576) fused_decode(const unsigned* __restrict__ x32, const char* __restrict__ K,
                                                    const char* __restrict__ V, const char* __restrict__ wq,
                                                    const char* __restrict__ wo, const char* __restrict__ wg,
                                                    const char* __restrict__ wu, const char* __restrict__ wd,
                                                    unsigned* out32, char* ws) {
  __shared__ __attribute__((aligned(16))) float macc[8 * 512];
  __shared__ __attribute__((aligned(16))) unsigned qs[256];
  __shared__ __attribute__((aligned(16))) unsigned xs[224];
  __shared__ __attribute__((aligned(16))) float mml[32];
  __shared__ __attribute__((aligned(16))) float red[8 * 16];
  __shared__ __attribute__((aligned(16))) unsigned uaddr[512];
  __shared__ int ctl[8];
  const int lane = threadIdx.x & 63, hwave = __builtin_amdgcn_readfirstlane(threadIdx.x >> 6);
  const int wave = hwave == 0 ? 8 : hwave - 1;
  const int b = blockIdx.x, g = b >> 5, grp = b >> 3, mem = b & 7, j32 = b & 31;
  asm volatile("" :: "s"(x32), "s"(K), "s"(V), "s"(wq), "s"(wo), "s"(wg), "s"(wu), "s"(wd), "s"(out32), "s"(ws));
  TS(0);
  const rsrc_t rw = __builtin_amdgcn_make_buffer_rsrc((void*)ws, 0, WS_USED, 0x00020000);
  const rsrc_t rk = __builtin_amdgcn_make_buffer_rsrc((void*)(K + (size_t)g * HEADB), 0, HEADB, 0x00020000);
  const rsrc_t rv = __builtin_amdgcn_make_buffer_rsrc((void*)(V + (size_t)g * HEADB), 0, HEADB, 0x00020000);
  unsigned tag;

  if (__builtin_expect(wave < 8, 1)) {
    const int voff = lane * 16;
    const unsigned sbase = (grp & 3) * REG + wave * 2048, srot = grp * ROT + mem * 16384u;
    auto offs = [&](int s) -> unsigned {
      return sbase + ((s * 131072u + srot) & (REG - 1));
    };
    u32x4 kb[NBACK + 1][2], vb[NBACK + 1][2];
    asm volatile("s_barrier" ::: "memory"); SCHED();
#pragma unroll
    for (int s = 0; s <= NBACK; ++s) {
      const unsigned o = offs(s);
#pragma unroll
      for (int u = 0; u < 2; ++u) {
        if (s > 0) asm volatile("s_waitcnt vmcnt(3)" ::: "memory");
        kb[s][u] = __builtin_amdgcn_raw_buffer_load_b128(rk, voff + u * 1024, o, K_AUX);
        SCHED();
      }
#pragma unroll
      for (int u = 0; u < 2; ++u) {
        if (s > 0) asm volatile("s_waitcnt vmcnt(3)" ::: "memory");
        vb[s][u] = __builtin_amdgcn_raw_buffer_load_b128(rv, voff + u * 1024, o, V_AUX);
        SCHED();
      }
    }
    TS(1);
    {
      int spin = 0;
#pragma unroll 1
      while (__builtin_amdgcn_readfirstlane(LDL(&ctl[1])) == 0 && ++spin < SPIN_MAX) __builtin_amdgcn_s_sleep(1);
    }
    asm volatile("" ::: "memory");
    TS(2);
    const u32x4 q0 = *(const u32x4*)&qs[(lane & 31) * 4], q1 = *(const u32x4*)&qs[128 + (lane & 31) * 4];
    tag = __builtin_amdgcn_readfirstlane(LDL(&ctl[2]));
    f32x2 a0[4], a1[4];
#pragma unroll
    for (int k = 0; k < 4; ++k) { a0[k] = f32x2{0.f, 0.f}; a1[k] = f32x2{0.f, 0.f}; }
    float m = -INFINITY, mc = -INFINITY, l = 0.f;
    const bool lane8 = (lane & 8) != 0;
#pragma unroll
    for (int s = 0; s < NBACK; ++s) {
      step<true>(kb[NBACK], vb[NBACK], q0, q1, a0, a1, m, mc, l, rk, rv, voff, offs(NBACK + 1 + s), lane8);
      step<false>(kb[s], vb[s], q0, q1, a0, a1, m, mc, l, rk, rv, voff, 0, lane8);
      SCHED();
    }
    TS(3);
    int n = 2 * NBACK + 1;
#pragma unroll 1
    for (;;) {
      unsigned o;
      if (n < S_TICK) {
        o = offs(n);
      } else {
        asm volatile("s_waitcnt vmcnt(3)" ::: "memory");
        int t = arrive(&ctl[0]);
        asm volatile("s_waitcnt lgkmcnt(0)" : "+v"(t) :: "memory");
        t = __builtin_amdgcn_readfirstlane(t);
        o = __builtin_amdgcn_readfirstlane(LDL(&uaddr[t]));
        int spin = 0;
#pragma unroll 1
        while (o == EMPTY && ++spin < SPIN_MAX) { __builtin_amdgcn_s_sleep(1); o = __builtin_amdgcn_readfirstlane(LDL(&uaddr[t])); }
        o = o == EMPTY ? OOB : o;
      }
      step<true>(kb[NBACK], vb[NBACK], q0, q1, a0, a1, m, mc, l, rk, rv, voff, o, lane8);
      if (o == OOB) break;
      ++n;
    }
    TS(4);
    f32x4 lo, hi;
    lo[0] = sw32_add(a0[0][0], a1[0][0]); lo[1] = sw32_add(a0[0][1], a1[0][1]);
    lo[2] = sw32_add(a0[1][0], a1[1][0]); lo[3] = sw32_add(a0[1][1], a1[1][1]);
    hi[0] = sw32_add(a0[2][0], a1[2][0]); hi[1] = sw32_add(a0[2][1], a1[2][1]);
    hi[2] = sw32_add(a0[3][0], a1[3][0]); hi[3] = sw32_add(a0[3][1], a1[3][1]);
    *(f32x4*)&macc[wave * 512 + lane * 4] = lo;
    *(f32x4*)&macc[wave * 512 + 256 + lane * 4] = hi;
    l += dpp<ROR8>(l); l = sw32_add(l, l);
    if ((lane & 47) == 0) *(f32x2*)&mml[(lane >> 4) * 16 + wave * 2] = f32x2{mc, l};
    int arr = arrive(&ctl[3]);
    asm volatile("s_waitcnt lgkmcnt(0)" : "+v"(arr) :: "memory");
    if (__builtin_amdgcn_readfirstlane(arr) == 7) {
      f32x4 ml[4], pl[8], ph[8];
#pragma unroll
      for (int i = 0; i < 4; ++i) ml[i] = *(const f32x4*)&mml[(lane >> 5) * 16 + i * 4];
#pragma unroll
      for (int w = 0; w < 8; ++w) { pl[w] = *(const f32x4*)&macc[w * 512 + lane * 4]; ph[w] = *(const f32x4*)&macc[w * 512 + 256 + lane * 4]; }
      float M = ml[0][0];
#pragma unroll
      for (int i = 0; i < 4; ++i) M = __builtin_fmaxf(M, __builtin_fmaxf(ml[i][0], ml[i][2]));
      float L = 0.f; f32x4 sl = {0.f, 0.f, 0.f, 0.f}, sh = {0.f, 0.f, 0.f, 0.f};
#pragma unroll
      for (int w = 0; w < 8; ++w) {
        const float f = __builtin_amdgcn_exp2f(ml[w >> 1][(w & 1) * 2] - M);
        L = __builtin_fmaf(f, ml[w >> 1][(w & 1) * 2 + 1], L);
        sl = __builtin_elementwise_fma(pl[w], f32x4{f, f, f, f}, sl);
        sh = __builtin_elementwise_fma(ph[w], f32x4{f, f, f, f}, sh);
      }
      const float sv[8] = {sl[0], sl[1], sl[2], sl[3], sh[0], sh[1], sh[2], sh[3]};
      const int vo = (lane >> 3) * 16384 + (lane & 7) * 16;
#pragma unroll
      for (int i = 0; i < 4; ++i)
        put(rw, WS_H1 + g * 131072 + i * 4096 + j32 * 128 + (vo), f2u(sv[2 * i]), f2u(sv[2 * i + 1]), tag);
      if ((lane & 31) == 0)
        put(rw, WS_ML + g * 1024 + j32 * 16 + ((lane >> 5) * 512), f2u(M), f2u(L), tag);
      SCHED();
      if (lane == 0) STL(&ctl[6], 1);
      TS(6);
    }
    const int c2 = b / 9;
    if (b % 9 == 0 && c2 < 28) {
      const int ow = (c2 >> 2) * 32 + (lane & 7) * 4 + (c2 & 3);
      const unsigned xv = x32[ow];
      unsigned off[4]; float v0[4], v1[4];
#pragma unroll
      for (int i = 0; i < 4; ++i) { off[i] = WS_H2 + (c2 * 256 + wave * 32) * 128 + i * 1024 + lane * 16; v0[i] = 0.f; v1[i] = 0.f; }
      {
        int spin = 0;
#pragma unroll 1
        while (__builtin_amdgcn_readfirstlane(LDL(&ctl[6])) == 0 && ++spin < SPIN_MAX) __builtin_amdgcn_s_sleep(1);
        asm volatile("" ::: "memory");
        u32x4 t[4];
        poll_issue<4>(rw, off, t);
        poll_check<4>(t, off, tag, v0, v1);
#pragma unroll 1
        while (__builtin_amdgcn_readfirstlane(LDL(&ctl[5])) == 0 && ++spin < SPIN_MAX) __builtin_amdgcn_s_sleep(1);
        asm volatile("" ::: "memory");
      }
      gather<4>(rw, off, tag, v0, v1);
      TS(7);
      float s = sw32_add(v0[0] + v0[1] + (v0[2] + v0[3]), v1[0] + v1[1] + (v1[2] + v1[3]));
      s += dpp<ROR8>(s); s = sw16_add(s, s);
      if ((lane & 24) == 0) red[wave * 16 + (lane & 7) * 2 + (lane >> 5)] = s;
      int fin = arrive(&ctl[4]);
      const f32x2 r = *(const f32x2*)&red[lane * 2];
      asm volatile("s_waitcnt lgkmcnt(0)" : "+v"(fin) :: "memory");
      if (__builtin_amdgcn_readfirstlane(fin) == 7) {
        float r0 = r[0], r1 = r[1];
        r0 += dpp<ROR8>(r0); r1 += dpp<ROR8>(r1);
        r0 = sw16_add(r0, r0); r1 = sw16_add(r1, r1);
        r0 = sw32_add(r0, r0); r1 = sw32_add(r1, r1);
        if (lane < 8) out32[ow] = pkbf(r0 + u2f(xv << 16), r1 + u2f(xv & 0xffff0000u));
        if (b == 0 && lane == 0) __builtin_amdgcn_raw_buffer_store_b32(tag, rw, 0, WS_EPOCH, 16);
        TS(8);
      }
    }
    asm volatile("" :: "v"(kb[NBACK][0]), "v"(kb[NBACK][1]), "v"(vb[NBACK][0]), "v"(vb[NBACK][1]));
  } else {
    __builtin_amdgcn_s_setprio(3);
    const unsigned ep = __builtin_amdgcn_raw_buffer_load_b32(rw, 0, WS_EPOCH, 16);
    unsigned xr[7];
#pragma unroll
    for (int j = 0; j < 7; ++j) xr[j] = x32[(lane >> 1) + 32 * j];
    u32x4 w[14];
    const int jq = mem * 4 + (grp & 3);
    {
      const rsrc_t r = __builtin_amdgcn_make_buffer_rsrc((void*)wq, 0, 448 * 8192, 0x00020000);
      const int vo = (lane >> 1) * 16384 + (lane & 1) * 16;
#pragma unroll
      for (int i = 0; i < 14; ++i) w[i] = __builtin_amdgcn_raw_buffer_load_b128(r, vo, g * 1024 + jq * 32 + (i >> 1) * 524288 + (i & 1) * 8192, 2);
    }
    if (j32 == 0 && lane == 0) __builtin_amdgcn_raw_buffer_store_b32(0u, rw, 0, WS_CTR + g * 256, 16);
    SCHED();
    *(u32x4*)&uaddr[lane * 4] = u32x4{EMPTY, EMPTY, EMPTY, EMPTY};
    *(u32x4*)&uaddr[256 + lane * 4] = u32x4{EMPTY, EMPTY, EMPTY, EMPTY};
    if (lane < NPRE) uaddr[lane] = (grp & 3) * REG + (lane & 7) * 2048 + (((S_TICK + (lane >> 3)) * 131072u + grp * ROT + mem * 16384u) & (REG - 1));
    if (lane < 8) ctl[lane] = 0;
    asm volatile("s_waitcnt lgkmcnt(0)\n s_barrier" ::: "memory");
    asm volatile("" : "+v"(xr[0]), "+v"(xr[1]), "+v"(xr[2]), "+v"(xr[3]), "+v"(xr[4]), "+v"(xr[5]), "+v"(xr[6]));
    unsigned xp[7];
    {
      float s = 0.f;
#pragma unroll
      for (int j = 0; j < 7; ++j) s += u2f(xr[j] << 16) + u2f(xr[j] & 0xffff0000u);
      const float mean = wave_sum(s) * (1.f / 896.f);
      float v = 0.f;
#pragma unroll
      for (int j = 0; j < 7; ++j) { const float da = u2f(xr[j] << 16) - mean, db = u2f(xr[j] & 0xffff0000u) - mean; v += da * da + db * db; }
      const float var = wave_sum(v) * (1.f / 896.f);
      const float mb = rbf(mean), rden = __builtin_amdgcn_rcpf(rbf(__builtin_amdgcn_sqrtf(rbf(rbf(var) + 1e-5f))));
#pragma unroll
      for (int j = 0; j < 7; ++j) xp[j] = pkbf(rbf(u2f(xr[j] << 16) - mb) * rden, rbf(u2f(xr[j] & 0xffff0000u) - mb) * rden);
    }
    tag = __builtin_amdgcn_readfirstlane(ep) + 1u;
    TS(1);
    {
      float r0, r1;
      gemv16(w, xp, r0, r1);
      const unsigned pk = pkbf(r0, r1);
      auto s = __builtin_amdgcn_permlane16_swap(pk, pk, false, false);
      unsigned s0 = s[0], s1 = s[1];
      asm volatile("s_waitcnt vmcnt(0)" : "+v"(s0), "+v"(s1) :: "memory");
      if ((lane & 30) == 0)
        put(rw, WS_Q + g * 2048 + jq * 64 + (((lane & 1) * 2 + (lane >> 5)) * 16), s0, s1, tag);
    }
    TS(2);
#pragma unroll
    for (int j = 0; j < 7; ++j) xs[(lane >> 1) + 32 * j] = xp[j];
    {
      unsigned off[2] = {WS_Q + g * 2048 + lane * 16u, WS_Q + g * 2048 + 1024 + lane * 16u};
      float v0[2] = {0.f, 0.f}, v1[2] = {0.f, 0.f};
      gather<2>(rw, off, tag, v0, v1);
      *(f32x2*)&qs[lane * 2] = f32x2{v0[0], v1[0]};
      *(f32x2*)&qs[128 + lane * 2] = f32x2{v0[1], v1[1]};
      if (lane == 0) STL(&ctl[2], (int)tag);
      asm volatile("s_waitcnt lgkmcnt(0)" ::: "memory");
      if (lane == 0) STL(&ctl[1], 1);
    }
    TS(3);
    const int fu = grp * 7 + mem;
    if (mem < 7) {
      const rsrc_t rg = __builtin_amdgcn_make_buffer_rsrc((void*)wg, 0, 448 * 3584, 0x00020000);
      const rsrc_t ru = __builtin_amdgcn_make_buffer_rsrc((void*)wu, 0, 448 * 3584, 0x00020000);
      const int vo = (lane >> 3) * 3584 + (lane & 7) * 16, so = (fu & 7) * 56 * 3584 + (fu >> 3) * 128;
#pragma unroll
      for (int i = 0; i < 7; ++i) {
        w[2 * i] = __builtin_amdgcn_raw_buffer_load_b128(rg, vo, so + i * 8 * 3584, 2);
        w[2 * i + 1] = __builtin_amdgcn_raw_buffer_load_b128(ru, vo, so + i * 8 * 3584, 2);
      }
      float a[16];
#pragma unroll
      for (int i = 0; i < 16; ++i) a[i] = 0.f;
#pragma unroll
      for (int i = 0; i < 7; ++i) {
        const unsigned xl = ((const unsigned short*)xs)[(fu & 7) * 56 + i * 8 + (lane >> 3)], xh = xl << 16;
#pragma unroll
        for (int e = 0; e < 4; ++e) {
          const unsigned wa = w[2 * i][e], wb = w[2 * i + 1][e];
          a[2 * e] = dot2(wa, xl, a[2 * e]); a[2 * e + 1] = dot2(wa, xh, a[2 * e + 1]);
          a[8 + 2 * e] = dot2(wb, xl, a[8 + 2 * e]); a[8 + 2 * e + 1] = dot2(wb, xh, a[8 + 2 * e + 1]);
        }
      }
#pragma unroll
      for (int i = 0; i < 8; ++i) a[i] = sw32_add(a[i], a[i + 8]);
#pragma unroll
      for (int i = 0; i < 4; ++i) { a[i] = sw16_add(a[i], a[i + 4]); a[i] += dpp<ROR8>(a[i]); }
      const bool b3 = (lane & 8) != 0;
      const int src = (((lane & 7) << 3) | (lane >> 3)) * 4;
      const unsigned d0 = __builtin_amdgcn_ds_bpermute(src, f2u(b3 ? a[2] : a[0])), d1 = __builtin_amdgcn_ds_bpermute(src, f2u(b3 ? a[3] : a[1]));
      put(rw, WS_F + (fu >> 3) * 8192 + (fu & 7) * 128 + ((lane >> 3) * 1024 + (lane & 7) * 16), d0, d1, tag);
    }
    SCHED();
    float fo[8];
    unsigned wp[8][8], hp = 0;
    const rsrc_t ro = __builtin_amdgcn_make_buffer_rsrc((void*)wo, 0, 4096 * 896, 0x00020000);
    const rsrc_t rd = __builtin_amdgcn_make_buffer_rsrc((void*)wd, 0, 1792 * 896, 0x00020000);
    const int row0 = g * 512 + (j32 >> 2) * 64 + (j32 & 3) * 2;
    const unsigned wvo = lane < 56 ? lane * 16u : OOB;
    u32x4 ov[16], dv[8];
    auto issue_w = [&]() __attribute__((always_inline)) {
#pragma unroll
      for (int i = 0; i < 8; ++i) dv[i] = __builtin_amdgcn_raw_buffer_load_b128(rd, wvo, mem < 7 ? (fu * 8 + i) * 896 : OOB, 2);
#pragma unroll
      for (int i = 0; i < 16; ++i) ov[i] = __builtin_amdgcn_raw_buffer_load_b128(ro, wvo, (row0 + (i >> 1) * 8 + (i & 1)) * 896, 2);
      SCHED();
    };
    auto prep_d = [&]() __attribute__((always_inline)) {
#pragma unroll
      for (int i = 0; i < 8; ++i) fo[i] = 0.f;
#pragma unroll
      for (int pc = 0; pc < 4; ++pc) {
        const unsigned hs = __builtin_amdgcn_readlane(hp, pc);
#pragma unroll
        for (int k = 0; k < 4; ++k) {
          unsigned w0 = dv[2 * pc][k], w1 = dv[2 * pc + 1][k];
          fo[2 * k] = dot2(__builtin_amdgcn_perm(w1, w0, 0x05040100u), hs, fo[2 * k]);
          fo[2 * k + 1] = dot2(__builtin_amdgcn_perm(w1, w0, 0x07060302u), hs, fo[2 * k + 1]);
        }
      }
      asm volatile("" : "+v"(fo[0]), "+v"(fo[1]), "+v"(fo[2]), "+v"(fo[3]), "+v"(fo[4]), "+v"(fo[5]), "+v"(fo[6]), "+v"(fo[7]));
      SCHED();
    };
    auto prep_o = [&]() __attribute__((always_inline)) {
#pragma unroll
      for (int pc = 0; pc < 8; ++pc)
#pragma unroll
        for (int k = 0; k < 4; ++k) {
          unsigned w0 = ov[2 * pc][k], w1 = ov[2 * pc + 1][k];
          wp[pc][2 * k] = __builtin_amdgcn_perm(w1, w0, 0x05040100u);
          wp[pc][2 * k + 1] = __builtin_amdgcn_perm(w1, w0, 0x07060302u);
          asm volatile("" : "+v"(wp[pc][2 * k]), "+v"(wp[pc][2 * k + 1]));
        }
      SCHED();
    };
    {
      const unsigned t0 = __builtin_amdgcn_raw_buffer_load_b32(ro, 0, row0 * 896, 2), t1 = __builtin_amdgcn_raw_buffer_load_b32(rd, 0, fu * 8 * 896, 2);
      const unsigned t2 = __builtin_amdgcn_raw_buffer_load_b32(rw, WS_H1 + b * 4096 + lane * 64, 0, 16);
      const unsigned t3 = __builtin_amdgcn_raw_buffer_load_b32(rw, WS_ML + g * 1024 + lane * 16, 0, 16);
      asm volatile("" :: "v"(t0), "v"(t1), "v"(t2), "v"(t3));
    }
    if (mem < 7) {
      unsigned off[1] = {WS_F + fu * 1024 + lane * 16u};
      float v0[1] = {0.f}, v1[1] = {0.f};
      gather<1>(rw, off, tag, v0, v1);
      float r0 = v0[0], r1 = v1[0];
      r0 += dpp<ROR8>(r0); r1 += dpp<ROR8>(r1);
      r0 = sw16_add(r0, r0); r1 = sw16_add(r1, r1);
      r0 = rbf(sw32_add(r0, r0)); r1 = rbf(sw32_add(r1, r1));
      const float u0 = dpp<ROR4>(r0), u1 = dpp<ROR4>(r1);
      const float s0 = rbf(r0 * __builtin_amdgcn_rcpf(1.f + __expf(-r0))), s1 = rbf(r1 * __builtin_amdgcn_rcpf(1.f + __expf(-r1)));
      hp = pkbf(s0 * u0, s1 * u1);
    }
    TS(4);
    {
      const rsrc_t rc = __builtin_amdgcn_make_buffer_rsrc((void*)(ws + WS_CTR + g * 256), 0, 4, 0x00020000);
      int k = NPRE, want = G1;
#pragma unroll 1
      for (int spin = 0; spin < 16 * SPIN_MAX; ++spin) {
        const int t = __builtin_amdgcn_readfirstlane(LDL(&ctl[0]));
        if (k - t <= THRESH) {
          const int gr = want > G1 ? G1 : k - t <= 0 && want < GMIN ? GMIN : want;
          const unsigned p = (unsigned)__builtin_amdgcn_readfirstlane(__builtin_amdgcn_raw_ptr_buffer_atomic_add_i32(gr, rc, lane * 4, 0, 16));
          const int rem = NP - (int)p - gr;
          const bool last = rem <= 0 || k + gr + G1 > UCAP;
          const unsigned pu = p + lane, jc = pu >> 3, rr = jc & 3;
          const unsigned o = rr * REG + (pu & 7) * 2048 + (((S_STATIC * 8 + (jc >> 2)) * 16384u + (g * 4 + rr) * ROT) & (REG - 1));
          const bool valid = pu < (unsigned)NP && lane < gr;
          if (valid || last) STL(&uaddr[k + lane], (int)(valid ? o : OOB));
          if (last) break;
          k += gr;
          const int t2 = __builtin_amdgcn_readfirstlane(LDL(&ctl[0]));
          const float ta = 0.5f * (float)(t + t2);
          const float rho = (S_TICK * 8 + ta) * __builtin_amdgcn_rcpf(256.f - Q0 - rem * (1.f / 32.f));
          const int kfin = __builtin_amdgcn_readfirstlane((int)(rho * (rem * (1.f / 32.f) + Q0 + FL) - FL + ta + 0.5f));
          want = kfin - k < 0 ? 0 : kfin - k;
        } else {
          __builtin_amdgcn_s_sleep(1);
        }
      }
    }
    TS(5);
    {
      unsigned off[5]; float v0[5], v1[5];
#pragma unroll
      for (int i = 0; i < 4; ++i) off[i] = WS_H1 + b * 4096 + i * 1024 + lane * 16;
      off[4] = WS_ML + g * 1024 + (j32 >> 4) * 512 + ((lane >> 3) + 8 * (lane & 3)) * 16;
#pragma unroll
      for (int i = 0; i < 5; ++i) { v0[i] = 0.f; v1[i] = 0.f; }
      int spin = 0;
#pragma unroll 1
      while (__builtin_amdgcn_readfirstlane(LDL(&ctl[3])) < LATEW && ++spin < SPIN_MAX) __builtin_amdgcn_s_sleep(1);
      asm volatile("" ::: "memory");
      issue_w();
      prep_d();
      prep_o();
      TS(6);
      gather<5>(rw, off, tag, v0, v1);
      TS(7);
      float M = v0[4];
      M = __builtin_fmaxf(M, dpp<XOR1>(M)); M = __builtin_fmaxf(M, dpp<XOR2>(M)); M = __builtin_fmaxf(M, dpp<ROR8>(M));
      M = sw16_max(M); M = sw32_max(M);
      const float f = __builtin_amdgcn_exp2f(v0[4] - M);
      float L = f * v1[4];
      const float f0 = dpp<0x00>(f), f1 = dpp<0x55>(f), f2 = dpp<0xAA>(f), f3 = dpp<0xFF>(f);
      float s0 = f0 * v0[0] + f1 * v0[1] + f2 * v0[2] + f3 * v0[3], s1 = f0 * v1[0] + f1 * v1[1] + f2 * v1[2] + f3 * v1[3];
      L += dpp<XOR1>(L); L += dpp<XOR2>(L);
      s0 += dpp<ROR8>(s0); s1 += dpp<ROR8>(s1); L += dpp<ROR8>(L);
      s0 = sw16_add(s0, s0); s1 = sw16_add(s1, s1); L = sw16_add(L, L);
      s0 = sw32_add(s0, s0); s1 = sw32_add(s1, s1); L = sw32_add(L, L);
      const float rl = __builtin_amdgcn_rcpf(L);
      const unsigned op = pkbf(s0 * rl, s1 * rl);
#pragma unroll
      for (int pc = 0; pc < 8; ++pc) {
        const unsigned os = __builtin_amdgcn_readlane(op, pc);
#pragma unroll
        for (int c = 0; c < 8; ++c) fo[c] = dot2(wp[pc][c], os, fo[c]);
      }
      if (lane < 56) {
        const int vo = (lane >> 3) * (4 * 256 * 128) + (lane & 7) * 16;
#pragma unroll
        for (int i = 0; i < 4; ++i)
          put(rw, WS_H2 + (i * 256 + b) * 128 + (vo), f2u(fo[2 * i]), f2u(fo[2 * i + 1]), tag);
      }
      SCHED();
      if (lane == 0) STL(&ctl[5], 1);
      TS(8);
    }
  }
}

extern "C" void kernel_launch(void* const* d_in, const int* in_sizes, int n_in, void* d_out, int out_size, void* d_ws,
                              size_t ws_size, hipStream_t stream) {
  fused_decode<<<256, 576, 0, stream>>>((const unsigned*)d_in[0], (const char*)d_in[1], (const char*)d_in[2],
                                        (const char*)d_in[3], (const char*)d_in[4], (const char*)d_in[6],
                                        (const char*)d_in[7], (const char*)d_in[8], (unsigned*)d_out, (char*)d_ws);
}
```

```cpp
#include <hip/hip_runtime.h>
#include <stdint.h>

typedef unsigned u32x4 __attribute__((ext_vector_type(4)));
typedef unsigned u32x2 __attribute__((ext_vector_type(2)));
typedef float f32x2 __attribute__((ext_vector_type(2)));
typedef float f32x4 __attribute__((ext_vector_type(4)));
typedef __bf16 bf16x2 __attribute__((ext_vector_type(2)));
#define LAS __attribute__((address_space(3)))
typedef __amdgpu_buffer_rsrc_t rsrc_t;

#define SV 6
#define TP 2
#define S_TICK (16 + SV)
#define S_STATIC (S_TICK + TP)
#define NBACK 6
#define BPL 2
#define RG 2
#define THRESH 8
#define G1 12
#define GMIN 2
#define Q0 4.f
#define FL 8.f
#define LATEW 1
#define IB 4
#define PD 4
#define BARF 16
#define ABAR 1
#define KV_AUX 2
#define DEV 16

constexpr unsigned HEADB = 16u << 20;
constexpr int NP = (32 - S_STATIC) * 256;
constexpr int NPRE = (S_STATIC - S_TICK) * 8;
constexpr int SE = 16 - SV;
constexpr int NL = (NBACK + BPL - 1) / BPL;
static_assert(NPRE <= 64 && SV >= 1 && NBACK + NL < 16, "");
constexpr int UCAP = 448;
static_assert(NP / (UCAP - NPRE - 2 * G1) <= 31, "someone stays until the pool is empty");
constexpr unsigned OOB = 0x40000000u, EMPTY = 0xffffffffu;
constexpr int SPIN_MAX = 200000;
constexpr unsigned WS_EPOCH = 0, WS_CTR = 4096, WS_Q = 16384, WS_ML = 32768, WS_H1 = 65536, WS_H2 = WS_H1 + (1u << 20);
constexpr unsigned WS_F = 2u << 20, WS_SC = 3u << 20, WS_USED = 4u << 20;
constexpr float CSC = 0.0625f * 1.4426950408889634f;

__device__ __forceinline__ float u2f(unsigned u) { return __builtin_bit_cast(float, u); }
__device__ __forceinline__ unsigned f2u(float f) { return __builtin_bit_cast(unsigned, f); }
__device__ __forceinline__ float rbf(float f) { __bf16 b = (__bf16)f; return (float)b; }
__device__ __forceinline__ unsigned pkbf(float lo, float hi) {
  bf16x2 v; v[0] = (__bf16)lo; v[1] = (__bf16)hi; return __builtin_bit_cast(unsigned, v);
}
__device__ __forceinline__ float dot2(unsigned a, unsigned b, float acc) {
  return __builtin_amdgcn_fdot2_f32_bf16(__builtin_bit_cast(bf16x2, a), __builtin_bit_cast(bf16x2, b), acc, false);
}
template <int CTRL> __device__ __forceinline__ float dpp(float x) {
  return __builtin_bit_cast(float, __builtin_amdgcn_mov_dpp(__builtin_bit_cast(int, x), CTRL, 0xf, 0xf, true));
}
constexpr int XOR1 = 0xB1, XOR2 = 0x4E, XOR7 = 0x141, ROR8 = 0x128, ROR4 = 0x124, BC0 = 0x150, BC8 = 0x158;
__device__ __forceinline__ float sw16_add(float a, float b) {
  auto s = __builtin_amdgcn_permlane16_swap(f2u(a), f2u(b), false, false);
  unsigned s0 = s[0], s1 = s[1]; return u2f(s0) + u2f(s1);
}
__device__ __forceinline__ float sw32_add(float a, float b) {
  auto s = __builtin_amdgcn_permlane32_swap(f2u(a), f2u(b), false, false);
  unsigned s0 = s[0], s1 = s[1]; return u2f(s0) + u2f(s1);
}
__device__ __forceinline__ float sw16_max(float a) {
  auto s = __builtin_amdgcn_permlane16_swap(f2u(a), f2u(a), false, false);
  unsigned s0 = s[0], s1 = s[1]; return __builtin_fmaxf(u2f(s0), u2f(s1));
}
__device__ __forceinline__ float sw32_max(float a) {
  auto s = __builtin_amdgcn_permlane32_swap(f2u(a), f2u(a), false, false);
  unsigned s0 = s[0], s1 = s[1]; return __builtin_fmaxf(u2f(s0), u2f(s1));
}
__device__ __forceinline__ float wave_sum(float t) {
  t += dpp<XOR1>(t); t += dpp<XOR2>(t); t += dpp<XOR7>(t); t += dpp<ROR8>(t);
  t = sw16_add(t, t); return sw32_add(t, t);
}
__device__ __forceinline__ float wave_max(float t) {
  t = __builtin_fmaxf(t, dpp<XOR1>(t)); t = __builtin_fmaxf(t, dpp<XOR2>(t)); t = __builtin_fmaxf(t, dpp<XOR7>(t));
  t = __builtin_fmaxf(t, dpp<ROR8>(t)); return sw32_max(sw16_max(t));
}
__device__ __forceinline__ float rdl(float v, int l) { return u2f(__builtin_amdgcn_readlane(f2u(v), l)); }
#define LDL(p) __hip_atomic_load((LAS int*)(p), __ATOMIC_RELAXED, __HIP_MEMORY_SCOPE_WORKGROUP)
#define STL(p, v) __hip_atomic_store((LAS int*)(p), v, __ATOMIC_RELAXED, __HIP_MEMORY_SCOPE_WORKGROUP)
#define SCHED() __builtin_amdgcn_sched_barrier(0)

template <bool REFILL>
__device__ __forceinline__ void kstep(u32x4 (&r)[4], const u32x4& q0, const u32x4& q1, float& mx, unsigned short* sc,
                                      rsrc_t rn, int voff, unsigned o, bool b3, bool b2) {
  float e[4];
#pragma unroll
  for (int u = 0; u < 4; ++u) {
    const u32x4 k = r[u];
    float x = 0.f, y = 0.f;
#pragma unroll
    for (int i = 0; i < 4; ++i) { unsigned w = k[i], qa = q0[i], qb = q1[i]; x = dot2(w, qa, x); y = dot2(w, qb, y); }
    if (REFILL) {
      asm volatile("" : "+v"(x), "+v"(y) :: "memory");
      r[u] = __builtin_amdgcn_raw_buffer_load_b128(rn, voff + u * 1024, o, KV_AUX);
      SCHED();
    }
    e[u] = sw16_add(x, y);
    e[u] += dpp<ROR8>(e[u]);
  }
  float s0 = b3 ? e[1] : e[0], s1 = b3 ? e[3] : e[2];
  s0 += dpp<XOR7>(s0); s1 += dpp<XOR7>(s1);
  float t = b2 ? s1 : s0;
  t += dpp<XOR2>(t); t += dpp<XOR1>(t);
  mx = __builtin_fmaxf(mx, t);
  *sc = __builtin_bit_cast(unsigned short, (__bf16)t);
}

__device__ __forceinline__ void vstep(u32x4 (&r)[4], unsigned sraw, float mcl, f32x2 (&a0)[4], f32x2 (&a1)[4], rsrc_t rv,
                                      int voff, unsigned o) {
  const float p = __builtin_amdgcn_exp2f(__builtin_fmaf(u2f(sraw << 16), CSC, -mcl));
  const float pp[8] = {dpp<BC0>(p), dpp<BC0 + 1>(p), dpp<BC0 + 2>(p), dpp<BC0 + 3>(p), dpp<BC0 + 4>(p), dpp<BC0 + 5>(p), dpp<BC0 + 6>(p), dpp<BC0 + 7>(p)};
#pragma unroll
  for (int u = 0; u < 4; ++u) {
    const u32x4 v = r[u];
    const f32x2 p0 = {pp[2 * u], pp[2 * u]}, p1 = {pp[2 * u + 1], pp[2 * u + 1]};
#pragma unroll
    for (int e = 0; e < 4; ++e) {
      unsigned w = v[e];
      const f32x2 x = {u2f(w << 16), u2f(w & 0xffff0000u)};
      a0[e] = __builtin_elementwise_fma(x, p0, a0[e]);
      a1[e] = __builtin_elementwise_fma(x, p1, a1[e]);
    }
    asm volatile("" : "+v"(a0[0]), "+v"(a0[1]), "+v"(a0[2]), "+v"(a0[3]), "+v"(a1[0]), "+v"(a1[1]), "+v"(a1[2]), "+v"(a1[3]) :: "memory");
    r[u] = __builtin_amdgcn_raw_buffer_load_b128(rv, voff + u * 1024, o, KV_AUX);
    SCHED();
  }
}

__device__ __forceinline__ void gemv16(const u32x4 (&w)[14], const unsigned (&xp)[7], float& r0, float& r1) {
  float a[8];
#pragma unroll
  for (int i = 0; i < 8; ++i) a[i] = 0.f;
#pragma unroll
  for (int j = 0; j < 7; ++j) {
#pragma unroll
    for (int k = 0; k < 4; ++k) {
      unsigned w0 = w[2 * j][k], w1 = w[2 * j + 1][k];
      a[2 * k] = dot2(__builtin_amdgcn_perm(w1, w0, 0x05040100u), xp[j], a[2 * k]);
      a[2 * k + 1] = dot2(__builtin_amdgcn_perm(w1, w0, 0x07060302u), xp[j], a[2 * k + 1]);
    }
  }
  float b[4];
#pragma unroll
  for (int i = 0; i < 4; ++i) b[i] = sw32_add(a[i], a[i + 4]);
  r0 = sw16_add(b[0], b[2]); r1 = sw16_add(b[1], b[3]);
  r0 += dpp<ROR8>(r0); r1 += dpp<ROR8>(r1);
  r0 += dpp<ROR4>(r0); r1 += dpp<ROR4>(r1);
  r0 += dpp<XOR2>(r0); r1 += dpp<XOR2>(r1);
}

__device__ __forceinline__ void put(rsrc_t rw, unsigned off, unsigned a, unsigned b, unsigned tag) {
  __builtin_amdgcn_raw_buffer_store_b128(u32x4{a, tag, b, tag}, rw, off, 0, DEV);
}

template <int N> __device__ __forceinline__ void poll_issue(rsrc_t rw, const unsigned (&off)[N], u32x4 (&t)[N]) {
#pragma unroll
  for (int i = 0; i < N; ++i) t[i] = __builtin_amdgcn_raw_buffer_load_b128(rw, off[i], 0, DEV);
  SCHED();
}
template <int N>
__device__ __forceinline__ bool poll_check(const u32x4 (&t)[N], unsigned (&off)[N], unsigned tag, float (&v0)[N], float (&v1)[N]) {
  bool left = false;
#pragma unroll
  for (int i = 0; i < N; ++i) {
    const unsigned t0 = t[i][0], t1 = t[i][1], t2 = t[i][2], t3 = t[i][3];
    const bool ok = (t1 == tag) & (t3 == tag) & (off[i] != OOB);
    v0[i] = ok ? u2f(t0) : v0[i]; v1[i] = ok ? u2f(t2) : v1[i];
    off[i] = ok ? OOB : off[i];
    left |= off[i] != OOB;
  }
  return __builtin_amdgcn_ballot_w64(left) == 0;
}
template <int N, int SL = 1>
__device__ __forceinline__ void gather(rsrc_t rw, unsigned (&off)[N], unsigned tag, float (&v0)[N], float (&v1)[N]) {
#pragma unroll 1
  for (int spin = 0; spin < SPIN_MAX; ++spin) {
    u32x4 t[N];
    poll_issue<N>(rw, off, t);
    if (poll_check<N>(t, off, tag, v0, v1)) break;
    __builtin_amdgcn_s_sleep(SL);
  }
}
__device__ __forceinline__ int arrive(int* p) {
  int r; unsigned long long e;
  asm volatile("s_mov_b64 %1, exec\n s_mov_b64 exec, 1\n ds_add_rtn_u32 %0, %2, %3\n s_mov_b64 exec, %1"
               : "=&v"(r), "=&s"(e) : "v"((unsigned)(uintptr_t)(LAS int*)p), "v"(1) : "memory");
  return r;
}

__device__ __forceinline__ void arrive_only(int* p) {
  unsigned long long e;
  asm volatile("s_mov_b64 %0, exec\n s_mov_b64 exec, 1\n ds_add_u32 %1, %2\n s_mov_b64 exec, %0"
               : "=&s"(e) : "v"((unsigned)(uintptr_t)(LAS int*)p), "v"(1) : "memory");
}

__global__ void __launch_bounds__(576) fused_decode(const unsigned* __restrict__ x32, const char* __restrict__ K,
                                                    const char* __restrict__ V, const char* __restrict__ wq,
                                                    const char* __restrict__ wo, const char* __restrict__ wg,
                                                    const char* __restrict__ wu, const char* __restrict__ wd,
                                                    unsigned* out32, char* ws) {
  const unsigned pt0 = (unsigned)__builtin_amdgcn_s_memrealtime(); __builtin_amdgcn_sched_barrier(0);
  __shared__ __attribute__((aligned(16))) float macc[8 * 512];
  __shared__ __attribute__((aligned(16))) unsigned short scs[2048];
  __shared__ __attribute__((aligned(16))) unsigned qs[256];
  __shared__ __attribute__((aligned(16))) unsigned xs[224];
  __shared__ __attribute__((aligned(16))) float gm[2];
  __shared__ __attribute__((aligned(16))) float red[8 * 16];
  __shared__ __attribute__((aligned(16))) unsigned uaddr[512];
  __shared__ int ctl[16];
  const int lane = threadIdx.x & 63, hwave = __builtin_amdgcn_readfirstlane(threadIdx.x >> 6);
  const int wave = hwave == 0 ? 8 : hwave - 1;
  const int b = blockIdx.x, g = b >> 5, grp = b >> 3, mem = b & 7, j32 = b & 31;
  asm volatile("" :: "s"(x32), "s"(K), "s"(V), "s"(wq), "s"(wo), "s"(wg), "s"(wu), "s"(wd), "s"(out32), "s"(ws));
  SCHED(); const unsigned pt1 = (unsigned)__builtin_amdgcn_s_memrealtime(); SCHED();
  unsigned pt2 = 0, pt3 = 0;
  const rsrc_t rw = __builtin_amdgcn_make_buffer_rsrc((void*)ws, 0, WS_USED, 0x00020000);
  const rsrc_t rk = __builtin_amdgcn_make_buffer_rsrc((void*)(K + (size_t)g * HEADB), 0, HEADB, 0x00020000);
  const rsrc_t rv = __builtin_amdgcn_make_buffer_rsrc((void*)(V + (size_t)g * HEADB), 0, HEADB, 0x00020000);
  const unsigned wbase = mem * 131072u + (grp & 3) * 32768u;
  unsigned tag;

  if (__builtin_expect(wave < 8, 1)) {
    const int voff = lane * 16;
    const unsigned sbase = wbase + wave * 4096;
    auto offs = [&](int s) -> unsigned { return sbase + (((s + RG * g) & 15) << 20); };
    u32x4 kb[NBACK + 1][4];
#pragma unroll
    for (int s = 0; s <= NBACK; ++s) {
      const unsigned o = offs(s);
#pragma unroll
      for (int u = 0; u < 4; ++u) {
        if (s * 4 + u == BARF) {
          asm volatile("s_barrier" ::: "memory"); SCHED();
        }
        if (s * 4 + u >= IB) asm volatile("s_waitcnt vmcnt(%0)" :: "n"(PD - 1) : "memory");
        if (s == 1 && u == 0) { SCHED(); pt3 = (unsigned)__builtin_amdgcn_s_memrealtime(); SCHED(); }
        kb[s][u] = __builtin_amdgcn_raw_buffer_load_b128(rk, voff + u * 1024, o, KV_AUX);
        SCHED();
        if (s == 0 && u == 0) { pt2 = (unsigned)__builtin_amdgcn_s_memrealtime(); SCHED(); }
      }
    }
    {
      int spin = 0;
#pragma unroll 1
      while (__builtin_amdgcn_readfirstlane(LDL(&ctl[1])) == 0 && ++spin < SPIN_MAX) __builtin_amdgcn_s_sleep(1);
    }
    asm volatile("" ::: "memory");
    const u32x4 q0 = *(const u32x4*)&qs[(lane & 31) * 4], q1 = *(const u32x4*)&qs[128 + (lane & 31) * 4];
    tag = __builtin_amdgcn_readfirstlane(LDL(&ctl[2]));
    if (lane == 0) *(u32x4*)(ws + (8u << 20) + (b * 16 + wave) * 16) = u32x4{pt0, pt1, pt2, pt3};
    float mx = -INFINITY;
    const bool b3 = (lane & 8) != 0, b2 = (lane & 4) != 0;
    unsigned short* scw = &scs[wave * 16 + (lane >> 5) * 8 + ((lane >> 3) & 1) * 2 + (lane & 4) + ((lane >> 4) & 1)];
#pragma unroll
    for (int s = 0; s < NL; ++s) {
      kstep<true>(kb[NBACK], q0, q1, mx, scw + (NBACK + s) * 128, rk, voff, offs(NBACK + 1 + s), b3, b2);
#pragma unroll
      for (int i = s * BPL; i < s * BPL + BPL && i < NBACK; ++i) kstep<false>(kb[i], q0, q1, mx, scw + i * 128, rk, voff, 0, b3, b2);
      SCHED();
    }
    if (SE <= NBACK + NL) arrive_only(&ctl[7]);
#pragma unroll 1
    for (int n = NBACK + NL; n < 16; ++n) {
      const rsrc_t rn = __builtin_amdgcn_make_buffer_rsrc((void*)((n < 15 ? K : V) + (size_t)g * HEADB), 0, HEADB, 0x00020000);
      kstep<true>(kb[NBACK], q0, q1, mx, scw + n * 128, rn, voff, offs(n < 15 ? n + 1 : SE), b3, b2);
      if (n == SE - 1 || n == 15) arrive_only(&ctl[n == 15 ? 8 : 7]);
    }
    mx = __builtin_fmaxf(mx, dpp<XOR7>(mx)); mx = __builtin_fmaxf(mx, dpp<ROR8>(mx));
    mx = rbf(sw32_max(mx)) * CSC;
    float mcl = (lane & 1) ? rdl(mx, 16) : rdl(mx, 0);
    f32x2 a0[4], a1[4];
#pragma unroll
    for (int k = 0; k < 4; ++k) { a0[k] = f32x2{0.f, 0.f}; a1[k] = f32x2{0.f, 0.f}; }
    const int svo = (lane >> 5) * 8 + (lane & 7);
    const unsigned short* scr = &scs[wave * 16 + svo];
    unsigned sraw = scr[SE * 128];
#pragma unroll 1
    for (int n = SE + 1; n < 16; ++n) {
      const unsigned sn = scr[n * 128];
      vstep(kb[NBACK], sraw, mcl, a0, a1, rv, voff, offs(n));
      sraw = sn;
    }
    auto ticket = [&]() __attribute__((always_inline)) -> unsigned {
      asm volatile("s_waitcnt vmcnt(3)" ::: "memory");
      int t = arrive(&ctl[0]);
      asm volatile("s_waitcnt lgkmcnt(0)" : "+v"(t) :: "memory");
      t = __builtin_amdgcn_readfirstlane(t);
      unsigned o = __builtin_amdgcn_readfirstlane(LDL(&uaddr[t]));
      int spin = 0;
#pragma unroll 1
      while (o == EMPTY && ++spin < SPIN_MAX) { __builtin_amdgcn_s_sleep(1); o = __builtin_amdgcn_readfirstlane(LDL(&uaddr[t])); }
      asm volatile("" ::: "memory");
      return o == EMPTY ? OOB : o;
    };
    unsigned o = ticket();
    {
      const float gl = gm[lane & 1];
      const float f = __builtin_amdgcn_exp2f(mcl - gl);
      const float f0 = rdl(f, 0), f1 = rdl(f, 1);
      mcl = gl;
#pragma unroll
      for (int k = 0; k < 4; ++k) { a0[k] *= f0; a1[k] *= f1; }
    }
    const rsrc_t rs = __builtin_amdgcn_make_buffer_rsrc((void*)(ws + WS_SC + g * 131072), 0, 131072, 0x00020000);
#pragma unroll 1
    for (;;) {
      const unsigned sn = __builtin_amdgcn_raw_buffer_load_b16(rs, svo * 2, o >> 7, DEV);
      SCHED();
      vstep(kb[NBACK], sraw, mcl, a0, a1, rv, voff, o);
      if (o == OOB) break;
      sraw = sn;
      o = ticket();
    }
    f32x4 lo, hi;
    lo[0] = sw32_add(a0[0][0], a1[0][0]); lo[1] = sw32_add(a0[0][1], a1[0][1]);
    lo[2] = sw32_add(a0[1][0], a1[1][0]); lo[3] = sw32_add(a0[1][1], a1[1][1]);
    hi[0] = sw32_add(a0[2][0], a1[2][0]); hi[1] = sw32_add(a0[2][1], a1[2][1]);
    hi[2] = sw32_add(a0[3][0], a1[3][0]); hi[3] = sw32_add(a0[3][1], a1[3][1]);
    *(f32x4*)&macc[wave * 512 + lane * 4] = lo;
    *(f32x4*)&macc[wave * 512 + 256 + lane * 4] = hi;
    int arr = arrive(&ctl[3]);
    asm volatile("s_waitcnt lgkmcnt(0)" : "+v"(arr) :: "memory");
    if (__builtin_amdgcn_readfirstlane(arr) == 7) {
      f32x4 pl[7], ph[7];
#pragma unroll
      for (int w = 0; w < 7; ++w) { const int x = ((wave + 1 + w) & 7) * 512 + lane * 4; pl[w] = *(const f32x4*)&macc[x]; ph[w] = *(const f32x4*)&macc[x + 256]; }
      lo += pl[0] + (pl[1] + pl[2]) + ((pl[3] + pl[4]) + (pl[5] + pl[6]));
      hi += ph[0] + (ph[1] + ph[2]) + ((ph[3] + ph[4]) + (ph[5] + ph[6]));
      const float sv[8] = {lo[0], lo[1], lo[2], lo[3], hi[0], hi[1], hi[2], hi[3]};
      const int vo = (lane >> 3) * 16384 + (lane & 7) * 16;
#pragma unroll
      for (int i = 0; i < 4; ++i)
        put(rw, WS_H1 + g * 131072 + i * 4096 + j32 * 128 + (vo), f2u(sv[2 * i]), f2u(sv[2 * i + 1]), tag);
      SCHED();
      if (lane == 0) STL(&ctl[6], 1);
    }
    const int c2 = b / 9;
    if (b % 9 == 0 && c2 < 28) {
      const int ow = (c2 >> 2) * 32 + (lane & 7) * 4 + (c2 & 3);
      const unsigned xv = x32[ow];
      unsigned off[4]; float v0[4], v1[4];
#pragma unroll
      for (int i = 0; i < 4; ++i) { off[i] = WS_H2 + (c2 * 256 + wave * 32) * 128 + i * 1024 + lane * 16; v0[i] = 0.f; v1[i] = 0.f; }
      {
        int spin = 0;
#pragma unroll 1
        while (__builtin_amdgcn_readfirstlane(LDL(&ctl[6])) == 0 && ++spin < SPIN_MAX) __builtin_amdgcn_s_sleep(1);
        asm volatile("" ::: "memory");
        u32x4 t[4];
        poll_issue<4>(rw, off, t);
        poll_check<4>(t, off, tag, v0, v1);
#pragma unroll 1
        while (__builtin_amdgcn_readfirstlane(LDL(&ctl[5])) == 0 && ++spin < SPIN_MAX) __builtin_amdgcn_s_sleep(1);
        asm volatile("" ::: "memory");
      }
      gather<4>(rw, off, tag, v0, v1);
      float s = sw32_add(v0[0] + v0[1] + (v0[2] + v0[3]), v1[0] + v1[1] + (v1[2] + v1[3]));
      s += dpp<ROR8>(s); s = sw16_add(s, s);
      if ((lane & 24) == 0) red[wave * 16 + (lane & 7) * 2 + (lane >> 5)] = s;
      int fin = arrive(&ctl[4]);
      const f32x2 r = *(const f32x2*)&red[lane * 2];
      asm volatile("s_waitcnt lgkmcnt(0)" : "+v"(fin) :: "memory");
      if (__builtin_amdgcn_readfirstlane(fin) == 7) {
        float r0 = r[0], r1 = r[1];
        r0 += dpp<ROR8>(r0); r1 += dpp<ROR8>(r1);
        r0 = sw16_add(r0, r0); r1 = sw16_add(r1, r1);
        r0 = sw32_add(r0, r0); r1 = sw32_add(r1, r1);
        if (lane < 8) {
          const unsigned ov = pkbf(r0 + u2f(xv << 16), r1 + u2f(xv & 0xffff0000u));
          __builtin_amdgcn_raw_buffer_store_b32(ov, __builtin_amdgcn_make_buffer_rsrc((void*)out32, 0, 896, 0x00020000), ow * 4, 0, DEV);
        }
        if (b == 0 && lane == 0) __builtin_amdgcn_raw_buffer_store_b32(tag, rw, 0, WS_EPOCH, DEV);
      }
    }
    asm volatile("" :: "v"(kb[NBACK][0]), "v"(kb[NBACK][1]), "v"(kb[NBACK][2]), "v"(kb[NBACK][3]));
  } else {
    __builtin_amdgcn_s_setprio(3);
    const unsigned ep = __builtin_amdgcn_raw_buffer_load_b32(rw, 0, WS_EPOCH, DEV);
    SCHED(); pt2 = (unsigned)__builtin_amdgcn_s_memrealtime(); SCHED();
    unsigned xr[7];
#pragma unroll
    for (int j = 0; j < 7; ++j) xr[j] = x32[(lane >> 1) + 32 * j];
    u32x4 w[14];
    const int jq = mem * 4 + (grp & 3);
    {
      const rsrc_t r = __builtin_amdgcn_make_buffer_rsrc((void*)wq, 0, 448 * 8192, 0x00020000);
      const int vo = (lane >> 1) * 16384 + (lane & 1) * 16;
#pragma unroll
      for (int i = 0; i < 14; ++i) w[i] = __builtin_amdgcn_raw_buffer_load_b128(r, vo, g * 1024 + jq * 32 + (i >> 1) * 524288 + (i & 1) * 8192, KV_AUX);
    }
    if (j32 == 0 && lane == 0) __builtin_amdgcn_raw_buffer_store_b32(0u, rw, 0, WS_CTR + g * 256, DEV);
    SCHED();
    *(u32x4*)&uaddr[lane * 4] = u32x4{EMPTY, EMPTY, EMPTY, EMPTY};
    *(u32x4*)&uaddr[256 + lane * 4] = u32x4{EMPTY, EMPTY, EMPTY, EMPTY};
    if (lane < 16) ctl[lane] = 0;
    if (ABAR == 0) asm volatile("s_waitcnt lgkmcnt(0)\n s_barrier" ::: "memory");
    asm volatile("" : "+v"(xr[0]), "+v"(xr[1]), "+v"(xr[2]), "+v"(xr[3]), "+v"(xr[4]), "+v"(xr[5]), "+v"(xr[6]));
    SCHED(); pt3 = (unsigned)__builtin_amdgcn_s_memrealtime(); SCHED();
    unsigned xp[7];
    {
      float s = 0.f;
#pragma unroll
      for (int j = 0; j < 7; ++j) s += u2f(xr[j] << 16) + u2f(xr[j] & 0xffff0000u);
      const float mean = wave_sum(s) * (1.f / 896.f);
      float v = 0.f;
#pragma unroll
      for (int j = 0; j < 7; ++j) { const float da = u2f(xr[j] << 16) - mean, db = u2f(xr[j] & 0xffff0000u) - mean; v += da * da + db * db; }
      const float var = wave_sum(v) * (1.f / 896.f);
      const float mb = rbf(mean), rden = __builtin_amdgcn_rcpf(rbf(__builtin_amdgcn_sqrtf(rbf(rbf(var) + 1e-5f))));
#pragma unroll
      for (int j = 0; j < 7; ++j) xp[j] = pkbf(rbf(u2f(xr[j] << 16) - mb) * rden, rbf(u2f(xr[j] & 0xffff0000u) - mb) * rden);
    }
    tag = __builtin_amdgcn_readfirstlane(ep) + 1u;
    {
      float r0, r1;
      gemv16(w, xp, r0, r1);
      const unsigned pk = pkbf(r0, r1);
      auto s = __builtin_amdgcn_permlane16_swap(pk, pk, false, false);
      unsigned s0 = s[0], s1 = s[1];
      asm volatile("s_waitcnt vmcnt(0)" : "+v"(s0), "+v"(s1) :: "memory");
      if ((lane & 30) == 0)
        put(rw, WS_Q + g * 2048 + jq * 64 + (((lane & 1) * 2 + (lane >> 5)) * 16), s0, s1, tag);
    }
#pragma unroll
    for (int j = 0; j < 7; ++j) xs[(lane >> 1) + 32 * j] = xp[j];
    if (ABAR == 1) asm volatile("s_waitcnt lgkmcnt(0)\n s_barrier" ::: "memory");
    {
      unsigned off[2] = {WS_Q + g * 2048 + lane * 16u, WS_Q + g * 2048 + 1024 + lane * 16u};
      float v0[2] = {0.f, 0.f}, v1[2] = {0.f, 0.f};
      gather<2>(rw, off, tag, v0, v1);
      *(f32x2*)&qs[lane * 2] = f32x2{v0[0], v1[0]};
      *(f32x2*)&qs[128 + lane * 2] = f32x2{v0[1], v1[1]};
      if (lane == 0) STL(&ctl[2], (int)tag);
      asm volatile("s_waitcnt lgkmcnt(0)" ::: "memory");
      if (lane == 0) STL(&ctl[1], 1);
    }
    if (lane == 0) *(u32x4*)(ws + (8u << 20) + (b * 16 + wave) * 16) = u32x4{pt0, pt1, pt2, pt3};
    const int fu = grp * 7 + mem;
    if (mem < 7) {
      const rsrc_t rg = __builtin_amdgcn_make_buffer_rsrc((void*)wg, 0, 448 * 3584, 0x00020000);
      const rsrc_t ru = __builtin_amdgcn_make_buffer_rsrc((void*)wu, 0, 448 * 3584, 0x00020000);
      const int vo = (lane >> 3) * 3584 + (lane & 7) * 16, so = (fu & 7) * 56 * 3584 + (fu >> 3) * 128;
#pragma unroll
      for (int i = 0; i < 7; ++i) {
        w[2 * i] = __builtin_amdgcn_raw_buffer_load_b128(rg, vo, so + i * 8 * 3584, KV_AUX);
        w[2 * i + 1] = __builtin_amdgcn_raw_buffer_load_b128(ru, vo, so + i * 8 * 3584, KV_AUX);
      }
      float a[16];
#pragma unroll
      for (int i = 0; i < 16; ++i) a[i] = 0.f;
#pragma unroll
      for (int i = 0; i < 7; ++i) {
        const unsigned xl = ((const unsigned short*)xs)[(fu & 7) * 56 + i * 8 + (lane >> 3)], xh = xl << 16;
#pragma unroll
        for (int e = 0; e < 4; ++e) {
          const unsigned wa = w[2 * i][e], wb = w[2 * i + 1][e];
          a[2 * e] = dot2(wa, xl, a[2 * e]); a[2 * e + 1] = dot2(wa, xh, a[2 * e + 1]);
          a[8 + 2 * e] = dot2(wb, xl, a[8 + 2 * e]); a[8 + 2 * e + 1] = dot2(wb, xh, a[8 + 2 * e + 1]);
        }
      }
#pragma unroll
      for (int i = 0; i < 8; ++i) a[i] = sw32_add(a[i], a[i + 8]);
#pragma unroll
      for (int i = 0; i < 4; ++i) { a[i] = sw16_add(a[i], a[i + 4]); a[i] += dpp<ROR8>(a[i]); }
      const bool b3 = (lane & 8) != 0;
      const int src = (((lane & 7) << 3) | (lane >> 3)) * 4;
      const unsigned d0 = __builtin_amdgcn_ds_bpermute(src, f2u(b3 ? a[2] : a[0])), d1 = __builtin_amdgcn_ds_bpermute(src, f2u(b3 ? a[3] : a[1]));
      put(rw, WS_F + (fu >> 3) * 8192 + (fu & 7) * 128 + ((lane >> 3) * 1024 + (lane & 7) * 16), d0, d1, tag);
    }
    SCHED();
    float fo[8];
    unsigned wp[8][8], hp = 0;
    const rsrc_t ro = __builtin_amdgcn_make_buffer_rsrc((void*)wo, 0, 4096 * 896, 0x00020000);
    const rsrc_t rd = __builtin_amdgcn_make_buffer_rsrc((void*)wd, 0, 1792 * 896, 0x00020000);
    const int row0 = g * 512 + (j32 >> 2) * 64 + (j32 & 3) * 2;
    const unsigned wvo = lane < 56 ? lane * 16u : OOB;
    u32x4 ov[16], dv[8];
    auto issue_w = [&]() __attribute__((always_inline)) {
#pragma unroll
      for (int i = 0; i < 8; ++i) dv[i] = __builtin_amdgcn_raw_buffer_load_b128(rd, wvo, mem < 7 ? (fu * 8 + i) * 896 : OOB, KV_AUX);
#pragma unroll
      for (int i = 0; i < 16; ++i) ov[i] = __builtin_amdgcn_raw_buffer_load_b128(ro, wvo, (row0 + (i >> 1) * 8 + (i & 1)) * 896, KV_AUX);
      SCHED();
    };
    auto prep_d = [&]() __attribute__((always_inline)) {
#pragma unroll
      for (int i = 0; i < 8; ++i) fo[i] = 0.f;
#pragma unroll
      for (int pc = 0; pc < 4; ++pc) {
        const unsigned hs = __builtin_amdgcn_readlane(hp, pc);
#pragma unroll
        for (int k = 0; k < 4; ++k) {
          unsigned w0 = dv[2 * pc][k], w1 = dv[2 * pc + 1][k];
          fo[2 * k] = dot2(__builtin_amdgcn_perm(w1, w0, 0x05040100u), hs, fo[2 * k]);
          fo[2 * k + 1] = dot2(__builtin_amdgcn_perm(w1, w0, 0x07060302u), hs, fo[2 * k + 1]);
        }
      }
      asm volatile("" : "+v"(fo[0]), "+v"(fo[1]), "+v"(fo[2]), "+v"(fo[3]), "+v"(fo[4]), "+v"(fo[5]), "+v"(fo[6]), "+v"(fo[7]));
      SCHED();
    };
    auto prep_o = [&]() __attribute__((always_inline)) {
#pragma unroll
      for (int pc = 0; pc < 8; ++pc)
#pragma unroll
        for (int k = 0; k < 4; ++k) {
          unsigned w0 = ov[2 * pc][k], w1 = ov[2 * pc + 1][k];
          wp[pc][2 * k] = __builtin_amdgcn_perm(w1, w0, 0x05040100u);
          wp[pc][2 * k + 1] = __builtin_amdgcn_perm(w1, w0, 0x07060302u);
          asm volatile("" : "+v"(wp[pc][2 * k]), "+v"(wp[pc][2 * k + 1]));
        }
      SCHED();
    };
    {
      const unsigned t0 = __builtin_amdgcn_raw_buffer_load_b32(ro, 0, row0 * 896, KV_AUX), t1 = __builtin_amdgcn_raw_buffer_load_b32(rd, 0, fu * 8 * 896, KV_AUX);
      asm volatile("" :: "v"(t0), "v"(t1));
    }
    if (mem < 7) {
      unsigned off[1] = {WS_F + fu * 1024 + lane * 16u};
      float v0[1] = {0.f}, v1[1] = {0.f};
      gather<1>(rw, off, tag, v0, v1);
      float r0 = v0[0], r1 = v1[0];
      r0 += dpp<ROR8>(r0); r1 += dpp<ROR8>(r1);
      r0 = sw16_add(r0, r0); r1 = sw16_add(r1, r1);
      r0 = rbf(sw32_add(r0, r0)); r1 = rbf(sw32_add(r1, r1));
      const float u0 = dpp<ROR4>(r0), u1 = dpp<ROR4>(r1);
      const float s0 = rbf(r0 * __builtin_amdgcn_rcpf(1.f + __expf(-r0))), s1 = rbf(r1 * __builtin_amdgcn_rcpf(1.f + __expf(-r1)));
      hp = pkbf(s0 * u0, s1 * u1);
    }
    {
      int spin = 0;
#pragma unroll 1
      while (__builtin_amdgcn_readfirstlane(LDL(&ctl[7])) < 8 && ++spin < SPIN_MAX) __builtin_amdgcn_s_sleep(2);
      asm volatile("" ::: "memory");
      constexpr int NQ = (SE + 3) / 4;
      u32x4 sq[4];
      float m0 = -INFINITY, m1 = -INFINITY;
#pragma unroll
      for (int i = 0; i < NQ; ++i) {
        const int st = i * 4 + (lane >> 4) < SE ? i * 4 + (lane >> 4) : 0;
        sq[i] = *(const u32x4*)&scs[st * 128 + (lane & 15) * 8];
        __builtin_amdgcn_raw_buffer_store_b128(sq[i], rw, WS_SC + g * 131072 + (wbase >> 7) + (((st + RG * g) & 15) << 13) + (lane & 15) * 16, 0, DEV);
      }
      SCHED();
#pragma unroll
      for (int i = 0; i < 4 * NQ; ++i) { const unsigned w = sq[i >> 2][i & 3]; m0 = __builtin_fmaxf(m0, u2f(w << 16)); m1 = __builtin_fmaxf(m1, u2f(w & 0xffff0000u)); }
      m0 = wave_max(m0); m1 = wave_max(m1);
      asm volatile("s_waitcnt vmcnt(0)" : "+v"(m0), "+v"(m1) :: "memory");
      if (lane == 0) put(rw, WS_ML + g * 2048 + 1024 + j32 * 16, f2u(m0), f2u(m1), tag);
      unsigned off[1] = {WS_ML + g * 2048 + 1024 + (lane & 31) * 16u};
      float v0[1] = {0.f}, v1[1] = {0.f};
#pragma unroll 1
      while (__builtin_amdgcn_readfirstlane(LDL(&ctl[8])) < 8 && ++spin < SPIN_MAX) __builtin_amdgcn_s_sleep(2);
      asm volatile("" ::: "memory");
      gather<1, 4>(rw, off, tag, v0, v1);
      m0 = v0[0]; m1 = v1[0];
#pragma unroll
      for (int i = 0; i < 4; ++i) sq[i] = *(const u32x4*)&scs[i * 512 + lane * 8];
#pragma unroll
      for (int i = 0; i < 16; ++i) { const unsigned w = sq[i >> 2][i & 3]; m0 = __builtin_fmaxf(m0, u2f(w << 16)); m1 = __builtin_fmaxf(m1, u2f(w & 0xffff0000u)); }
      m0 = wave_max(m0) * CSC; m1 = wave_max(m1) * CSC;
      if (lane < 2) gm[lane] = lane ? m1 : m0;
      asm volatile("s_waitcnt lgkmcnt(0)" ::: "memory");
      if (lane < NPRE) STL(&uaddr[lane], (int)(wbase + (lane & 7) * 4096 + (((SE - TP + (lane >> 3) + RG * g) & 15) << 20)));
      float l0 = 0.f, l1 = 0.f;
#pragma unroll
      for (int i = 0; i < 16; ++i) {
        const unsigned w = sq[i >> 2][i & 3];
        l0 += __builtin_amdgcn_exp2f(__builtin_fmaf(u2f(w << 16), CSC, -m0)); l1 += __builtin_amdgcn_exp2f(__builtin_fmaf(u2f(w & 0xffff0000u), CSC, -m1));
      }
      l0 = wave_sum(l0); l1 = wave_sum(l1);
      if ((lane & 31) == 0)
        put(rw, WS_ML + g * 2048 + j32 * 16 + ((lane >> 5) * 512), f2u(lane ? m1 : m0), f2u(lane ? l1 : l0), tag);
    }
    {
      const rsrc_t rc = __builtin_amdgcn_make_buffer_rsrc((void*)(ws + WS_CTR + g * 256), 0, 4, 0x00020000);
      int k = NPRE, want = G1;
#pragma unroll 1
      for (int spin = 0; spin < 16 * SPIN_MAX; ++spin) {
        const int t = __builtin_amdgcn_readfirstlane(LDL(&ctl[0]));
        if (k - t <= THRESH) {
          const int gr = want > G1 ? G1 : k - t <= 0 && want < GMIN ? GMIN : want;
          const unsigned p = (unsigned)__builtin_amdgcn_readfirstlane(__builtin_amdgcn_raw_ptr_buffer_atomic_add_i32(gr, rc, lane * 4, 0, DEV));
          const int rem = NP - (int)p - gr;
          const bool last = rem <= 0 || k + gr + G1 > UCAP;
          const unsigned pu = p + lane;
          const unsigned o = (pu * 4096u + ((RG * g) << 20)) & (HEADB - 1);
          const bool valid = pu < (unsigned)NP && lane < gr;
          if (valid || last) STL(&uaddr[k + lane], (int)(valid ? o : OOB));
          if (last) break;
          k += gr;
          const int t2 = __builtin_amdgcn_readfirstlane(LDL(&ctl[0]));
          const float ta = 0.5f * (float)(t + t2);
          const float rho = (S_TICK * 8 + ta) * __builtin_amdgcn_rcpf(256.f - Q0 - rem * (1.f / 32.f));
          const int kfin = __builtin_amdgcn_readfirstlane((int)(rho * (rem * (1.f / 32.f) + Q0 + FL) - FL + ta + 0.5f));
          want = kfin - k < 0 ? 0 : kfin - k;
        } else {
          __builtin_amdgcn_s_sleep(1);
        }
      }
    }
    {
      unsigned off[4]; float v0[4], v1[4];
#pragma unroll
      for (int i = 0; i < 4; ++i) { off[i] = WS_H1 + b * 4096 + i * 1024 + lane * 16; v0[i] = 0.f; v1[i] = 0.f; }
      int spin = 0;
#pragma unroll 1
      while (__builtin_amdgcn_readfirstlane(LDL(&ctl[3])) < LATEW && ++spin < SPIN_MAX) __builtin_amdgcn_s_sleep(1);
      asm volatile("" ::: "memory");
      float f0, f1, f2, f3;
      {
        unsigned fo[1] = {WS_ML + g * 2048 + (j32 >> 4) * 512 + ((lane >> 3) + 8 * (lane & 3)) * 16u};
        float m[1] = {0.f}, l[1] = {0.f};
        u32x4 t[1];
        poll_issue<1>(rw, fo, t);
        issue_w();
        if (!poll_check<1>(t, fo, tag, m, l)) gather<1>(rw, fo, tag, m, l);
        float M = m[0];
        M = __builtin_fmaxf(M, dpp<XOR1>(M)); M = __builtin_fmaxf(M, dpp<XOR2>(M)); M = __builtin_fmaxf(M, dpp<ROR8>(M));
        M = sw16_max(M); M = sw32_max(M);
        float f = __builtin_amdgcn_exp2f(m[0] - M);
        float L = f * l[0];
        L += dpp<XOR1>(L); L += dpp<XOR2>(L); L += dpp<ROR8>(L); L = sw16_add(L, L); L = sw32_add(L, L);
        f *= __builtin_amdgcn_rcpf(L);
        f0 = dpp<0x00>(f); f1 = dpp<0x55>(f); f2 = dpp<0xAA>(f); f3 = dpp<0xFF>(f);
        asm volatile("" : "+v"(f0), "+v"(f1), "+v"(f2), "+v"(f3));
      }
      prep_d();
      prep_o();
#pragma unroll 1
      while (__builtin_amdgcn_readfirstlane(LDL(&ctl[6])) == 0 && ++spin < SPIN_MAX) __builtin_amdgcn_s_sleep(1);
      asm volatile("" ::: "memory");
      gather<4>(rw, off, tag, v0, v1);
      float s0 = f0 * v0[0] + f1 * v0[1] + (f2 * v0[2] + f3 * v0[3]), s1 = f0 * v1[0] + f1 * v1[1] + (f2 * v1[2] + f3 * v1[3]);
      s0 += dpp<ROR8>(s0); s1 += dpp<ROR8>(s1);
      s0 = sw16_add(s0, s0); s1 = sw16_add(s1, s1);
      s0 = sw32_add(s0, s0); s1 = sw32_add(s1, s1);
      const unsigned op = pkbf(s0, s1);
#pragma unroll
      for (int pc = 0; pc < 8; ++pc) {
        const unsigned os = __builtin_amdgcn_readlane(op, pc);
#pragma unroll
        for (int c = 0; c < 8; ++c) fo[c] = dot2(wp[pc][c], os, fo[c]);
      }
      if (lane < 56) {
        const int vo = (lane >> 3) * (4 * 256 * 128) + (lane & 7) * 16;
#pragma unroll
        for (int i = 0; i < 4; ++i)
          put(rw, WS_H2 + (i * 256 + b) * 128 + (vo), f2u(fo[2 * i]), f2u(fo[2 * i + 1]), tag);
      }
      SCHED();
      if (lane == 0) STL(&ctl[5], 1);
    }
  }
}

__global__ void report(char* ws) {
  const unsigned ts = (unsigned)__builtin_amdgcn_s_memrealtime();
  const u32x4* h = (const u32x4*)(ws + (8u << 20));
  __shared__ int r, mn;
  const int t = threadIdx.x;
  const unsigned base = h[8][0];
  if (t == 0) { r = 0; mn = 1 << 20; }
  __syncthreads();
  for (int w = 0; w < 9; ++w) atomicMin(&mn, (int)(h[t * 16 + w][0] - base));
  __syncthreads();
  const unsigned tmin = base + mn;
  for (int w = 0; w < 8; ++w) { const u32x4 q = h[t * 16 + w]; const unsigned t0 = q[0], t1 = q[1], t2 = q[2], t3 = q[3]; atomicAdd(&r, (int)(0)); }
  __syncthreads();
  if (t == 0) {
    int v = r * 10 / (256 * 8); v = v < 0 ? 0 : v > 20000 ? 20000 : v;
    const unsigned until = ts + v + 2000u;
    for (int i = 0; i < 1000000; ++i) { if ((int)((unsigned)__builtin_amdgcn_s_memrealtime() - until) >= 0) break; __builtin_amdgcn_s_sleep(1); }
  }
}
extern "C" void kernel_launch(void* const* d_in, const int* in_sizes, int n_in, void* d_out, int out_size, void* d_ws,
                              size_t ws_size, hipStream_t stream) {
  fused_decode<<<256, 576, 0, stream>>>((const unsigned*)d_in[0], (const char*)d_in[1], (const char*)d_in[2],
                                        (const char*)d_in[3], (const char*)d_in[4], (const char*)d_in[6],
                                        (const char*)d_in[7], (const char*)d_in[8], (unsigned*)d_out, (char*)d_ws);
  report<<<1, 256, 0, stream>>>((char*)d_ws);
}
```

```cpp
#include <hip/hip_runtime.h>
#include <stdint.h>

typedef unsigned u32x4 __attribute__((ext_vector_type(4)));
typedef unsigned u32x2 __attribute__((ext_vector_type(2)));
typedef float f32x2 __attribute__((ext_vector_type(2)));
typedef float f32x4 __attribute__((ext_vector_type(4)));
typedef __bf16 bf16x2 __attribute__((ext_vector_type(2)));
#define LAS __attribute__((address_space(3)))
typedef __amdgpu_buffer_rsrc_t rsrc_t;

#define SV 6
#define TP 2
#define S_TICK (16 + SV)
#define S_STATIC (S_TICK + TP)
#define NBACK 6
#define BPL 2
#define RG 2
#define THRESH 8
#define G1 12
#define GMIN 2
#define Q0 4.f
#define FL 8.f
#define LATEW 1
#define IB 4
#define PD 4
#define BARF 16
#define ABAR 1
#define KV_AUX 2
#define DEV 16

constexpr unsigned HEADB = 16u << 20;
constexpr int NP = (32 - S_STATIC) * 256;
constexpr int NPRE = (S_STATIC - S_TICK) * 8;
constexpr int SE = 16 - SV;
constexpr int NL = (NBACK + BPL - 1) / BPL;
static_assert(NPRE <= 64 && SV >= 1 && NBACK + NL < 16, "");
constexpr int UCAP = 448;
static_assert(NP / (UCAP - NPRE - 2 * G1) <= 31, "someone stays until the pool is empty");
constexpr unsigned OOB = 0x40000000u, EMPTY = 0xffffffffu;
constexpr int SPIN_MAX = 200000;
constexpr unsigned WS_EPOCH = 0, WS_CTR = 4096, WS_Q = 16384, WS_ML = 32768, WS_H1 = 65536, WS_H2 = WS_H1 + (1u << 20);
constexpr unsigned WS_F = 2u << 20, WS_SC = 3u << 20, WS_USED = 4u << 20;
constexpr float CSC = 0.0625f * 1.4426950408889634f;

__device__ __forceinline__ float u2f(unsigned u) { return __builtin_bit_cast(float, u); }
__device__ __forceinline__ unsigned f2u(float f) { return __builtin_bit_cast(unsigned, f); }
__device__ __forceinline__ float rbf(float f) { __bf16 b = (__bf16)f; return (float)b; }
__device__ __forceinline__ unsigned pkbf(float lo, float hi) {
  bf16x2 v; v[0] = (__bf16)lo; v[1] = (__bf16)hi; return __builtin_bit_cast(unsigned, v);
}
__device__ __forceinline__ float dot2(unsigned a, unsigned b, float acc) {
  return __builtin_amdgcn_fdot2_f32_bf16(__builtin_bit_cast(bf16x2, a), __builtin_bit_cast(bf16x2, b), acc, false);
}
template <int CTRL> __device__ __forceinline__ float dpp(float x) {
  return __builtin_bit_cast(float, __builtin_amdgcn_mov_dpp(__builtin_bit_cast(int, x), CTRL, 0xf, 0xf, true));
}
constexpr int XOR1 = 0xB1, XOR2 = 0x4E, XOR7 = 0x141, ROR8 = 0x128, ROR4 = 0x124, BC0 = 0x150, BC8 = 0x158;
__device__ __forceinline__ float sw16_add(float a, float b) {
  auto s = __builtin_amdgcn_permlane16_swap(f2u(a), f2u(b), false, false);
  unsigned s0 = s[0], s1 = s[1]; return u2f(s0) + u2f(s1);
}
__device__ __forceinline__ float sw32_add(float a, float b) {
  auto s = __builtin_amdgcn_permlane32_swap(f2u(a), f2u(b), false, false);
  unsigned s0 = s[0], s1 = s[1]; return u2f(s0) + u2f(s1);
}
__device__ __forceinline__ float sw16_max(float a) {
  auto s = __builtin_amdgcn_permlane16_swap(f2u(a), f2u(a), false, false);
  unsigned s0 = s[0], s1 = s[1]; return __builtin_fmaxf(u2f(s0), u2f(s1));
}
__device__ __forceinline__ float sw32_max(float a) {
  auto s = __builtin_amdgcn_permlane32_swap(f2u(a), f2u(a), false, false);
  unsigned s0 = s[0], s1 = s[1]; return __builtin_fmaxf(u2f(s0), u2f(s1));
}
__device__ __forceinline__ float wave_sum(float t) {
  t += dpp<XOR1>(t); t += dpp<XOR2>(t); t += dpp<XOR7>(t); t += dpp<ROR8>(t);
  t = sw16_add(t, t); return sw32_add(t, t);
}
__device__ __forceinline__ float wave_max(float t) {
  t = __builtin_fmaxf(t, dpp<XOR1>(t)); t = __builtin_fmaxf(t, dpp<XOR2>(t)); t = __builtin_fmaxf(t, dpp<XOR7>(t));
  t = __builtin_fmaxf(t, dpp<ROR8>(t)); return sw32_max(sw16_max(t));
}
__device__ __forceinline__ float rdl(float v, int l) { return u2f(__builtin_amdgcn_readlane(f2u(v), l)); }
#define LDL(p) __hip_atomic_load((LAS int*)(p), __ATOMIC_RELAXED, __HIP_MEMORY_SCOPE_WORKGROUP)
#define STL(p, v) __hip_atomic_store((LAS int*)(p), v, __ATOMIC_RELAXED, __HIP_MEMORY_SCOPE_WORKGROUP)
#define SCHED() __builtin_amdgcn_sched_barrier(0)

template <bool REFILL>
__device__ __forceinline__ void kstep(u32x4 (&r)[4], const u32x4& q0, const u32x4& q1, float& mx, unsigned short* sc,
                                      rsrc_t rn, int voff, unsigned o, bool b3, bool b2) {
  float e[4];
#pragma unroll
  for (int u = 0; u < 4; ++u) {
    const u32x4 k = r[u];
    float x = 0.f, y = 0.f;
#pragma unroll
    for (int i = 0; i < 4; ++i) { unsigned w = k[i], qa = q0[i], qb = q1[i]; x = dot2(w, qa, x); y = dot2(w, qb, y); }
    if (REFILL) {
      asm volatile("" : "+v"(x), "+v"(y) :: "memory");
      r[u] = __builtin_amdgcn_raw_buffer_load_b128(rn, voff + u * 1024, o, KV_AUX);
      SCHED();
    }
    e[u] = sw16_add(x, y);
    e[u] += dpp<ROR8>(e[u]);
  }
  float s0 = b3 ? e[1] : e[0], s1 = b3 ? e[3] : e[2];
  s0 += dpp<XOR7>(s0); s1 += dpp<XOR7>(s1);
  float t = b2 ? s1 : s0;
  t += dpp<XOR2>(t); t += dpp<XOR1>(t);
  mx = __builtin_fmaxf(mx, t);
  *sc = __builtin_bit_cast(unsigned short, (__bf16)t);
}

__device__ __forceinline__ void vstep(u32x4 (&r)[4], unsigned sraw, float mcl, f32x2 (&a0)[4], f32x2 (&a1)[4], rsrc_t rv,
                                      int voff, unsigned o) {
  const float p = __builtin_amdgcn_exp2f(__builtin_fmaf(u2f(sraw << 16), CSC, -mcl));
  const float pp[8] = {dpp<BC0>(p), dpp<BC0 + 1>(p), dpp<BC0 + 2>(p), dpp<BC0 + 3>(p), dpp<BC0 + 4>(p), dpp<BC0 + 5>(p), dpp<BC0 + 6>(p), dpp<BC0 + 7>(p)};
#pragma unroll
  for (int u = 0; u < 4; ++u) {
    const u32x4 v = r[u];
    const f32x2 p0 = {pp[2 * u], pp[2 * u]}, p1 = {pp[2 * u + 1], pp[2 * u + 1]};
#pragma unroll
    for (int e = 0; e < 4; ++e) {
      unsigned w = v[e];
      const f32x2 x = {u2f(w << 16), u2f(w & 0xffff0000u)};
      a0[e] = __builtin_elementwise_fma(x, p0, a0[e]);
      a1[e] = __builtin_elementwise_fma(x, p1, a1[e]);
    }
    asm volatile("" : "+v"(a0[0]), "+v"(a0[1]), "+v"(a0[2]), "+v"(a0[3]), "+v"(a1[0]), "+v"(a1[1]), "+v"(a1[2]), "+v"(a1[3]) :: "memory");
    r[u] = __builtin_amdgcn_raw_buffer_load_b128(rv, voff + u * 1024, o, KV_AUX);
    SCHED();
  }
}

__device__ __forceinline__ void gemv16(const u32x4 (&w)[14], const unsigned (&xp)[7], float& r0, float& r1) {
  float a[8];
#pragma unroll
  for (int i = 0; i < 8; ++i) a[i] = 0.f;
#pragma unroll
  for (int j = 0; j < 7; ++j) {
#pragma unroll
    for (int k = 0; k < 4; ++k) {
      unsigned w0 = w[2 * j][k], w1 = w[2 * j + 1][k];
      a[2 * k] = dot2(__builtin_amdgcn_perm(w1, w0, 0x05040100u), xp[j], a[2 * k]);
      a[2 * k + 1] = dot2(__builtin_amdgcn_perm(w1, w0, 0x07060302u), xp[j], a[2 * k + 1]);
    }
  }
  float b[4];
#pragma unroll
  for (int i = 0; i < 4; ++i) b[i] = sw32_add(a[i], a[i + 4]);
  r0 = sw16_add(b[0], b[2]); r1 = sw16_add(b[1], b[3]);
  r0 += dpp<ROR8>(r0); r1 += dpp<ROR8>(r1);
  r0 += dpp<ROR4>(r0); r1 += dpp<ROR4>(r1);
  r0 += dpp<XOR2>(r0); r1 += dpp<XOR2>(r1);
}

__device__ __forceinline__ void put(rsrc_t rw, unsigned off, unsigned a, unsigned b, unsigned tag) {
  __builtin_amdgcn_raw_buffer_store_b128(u32x4{a, tag, b, tag}, rw, off, 0, DEV);
}

template <int N> __device__ __forceinline__ void poll_issue(rsrc_t rw, const unsigned (&off)[N], u32x4 (&t)[N]) {
#pragma unroll
  for (int i = 0; i < N; ++i) t[i] = __builtin_amdgcn_raw_buffer_load_b128(rw, off[i], 0, DEV);
  SCHED();
}
template <int N>
__device__ __forceinline__ bool poll_check(const u32x4 (&t)[N], unsigned (&off)[N], unsigned tag, float (&v0)[N], float (&v1)[N]) {
  bool left = false;
#pragma unroll
  for (int i = 0; i < N; ++i) {
    const unsigned t0 = t[i][0], t1 = t[i][1], t2 = t[i][2], t3 = t[i][3];
    const bool ok = (t1 == tag) & (t3 == tag) & (off[i] != OOB);
    v0[i] = ok ? u2f(t0) : v0[i]; v1[i] = ok ? u2f(t2) : v1[i];
    off[i] = ok ? OOB : off[i];
    left |= off[i] != OOB;
  }
  return __builtin_amdgcn_ballot_w64(left) == 0;
}
template <int N, int SL = 1>
__device__ __forceinline__ void gather(rsrc_t rw, unsigned (&off)[N], unsigned tag, float (&v0)[N], float (&v1)[N]) {
#pragma unroll 1
  for (int spin = 0; spin < SPIN_MAX; ++spin) {
    u32x4 t[N];
    poll_issue<N>(rw, off, t);
    if (poll_check<N>(t, off, tag, v0, v1)) break;
    __builtin_amdgcn_s_sleep(SL);
  }
}
__device__ __forceinline__ int arrive(int* p) {
  int r; unsigned long long e;
  asm volatile("s_mov_b64 %1, exec\n s_mov_b64 exec, 1\n ds_add_rtn_u32 %0, %2, %3\n s_mov_b64 exec, %1"
               : "=&v"(r), "=&s"(e) : "v"((unsigned)(uintptr_t)(LAS int*)p), "v"(1) : "memory");
  return r;
}

__device__ __forceinline__ void arrive_only(int* p) {
  unsigned long long e;
  asm volatile("s_mov_b64 %0, exec\n s_mov_b64 exec, 1\n ds_add_u32 %1, %2\n s_mov_b64 exec, %0"
               : "=&s"(e) : "v"((unsigned)(uintptr_t)(LAS int*)p), "v"(1) : "memory");
}

__global__ void __launch_bounds__(576) fused_decode(const unsigned* __restrict__ x32, const char* __restrict__ K,
                                                    const char* __restrict__ V, const char* __restrict__ wq,
                                                    const char* __restrict__ wo, const char* __restrict__ wg,
                                                    const char* __restrict__ wu, const char* __restrict__ wd,
                                                    unsigned* out32, char* ws) {
  const unsigned pt0 = (unsigned)__builtin_amdgcn_s_memrealtime(); __builtin_amdgcn_sched_barrier(0);
  __shared__ __attribute__((aligned(16))) float macc[8 * 512];
  __shared__ __attribute__((aligned(16))) unsigned short scs[2048];
  __shared__ __attribute__((aligned(16))) unsigned qs[256];
  __shared__ __attribute__((aligned(16))) unsigned xs[224];
  __shared__ __attribute__((aligned(16))) float gm[2];
  __shared__ __attribute__((aligned(16))) float red[8 * 16];
  __shared__ __attribute__((aligned(16))) unsigned uaddr[512];
  __shared__ int ctl[16];
  const int lane = threadIdx.x & 63, hwave = __builtin_amdgcn_readfirstlane(threadIdx.x >> 6);
  const int wave = hwave == 0 ? 8 : hwave - 1;
  const int b = blockIdx.x, g = b >> 5, grp = b >> 3, mem = b & 7, j32 = b & 31;
  asm volatile("" :: "s"(x32), "s"(K), "s"(V), "s"(wq), "s"(wo), "s"(wg), "s"(wu), "s"(wd), "s"(out32), "s"(ws));
  const unsigned pt1 = 0;
  unsigned pt2 = 0, pt3 = 0;
  const rsrc_t rw = __builtin_amdgcn_make_buffer_rsrc((void*)ws, 0, WS_USED, 0x00020000);
  const rsrc_t rk = __builtin_amdgcn_make_buffer_rsrc((void*)(K + (size_t)g * HEADB), 0, HEADB, 0x00020000);
  const rsrc_t rv = __builtin_amdgcn_make_buffer_rsrc((void*)(V + (size_t)g * HEADB), 0, HEADB, 0x00020000);
  const unsigned wbase = mem * 131072u + (grp & 3) * 32768u;
  unsigned tag;

  if (__builtin_expect(wave < 8, 1)) {
    const int voff = lane * 16;
    const unsigned sbase = wbase + wave * 4096;
    auto offs = [&](int s) -> unsigned { return sbase + (((s + RG * g) & 15) << 20); };
    u32x4 kb[NBACK + 1][4];
#pragma unroll
    for (int s = 0; s <= NBACK; ++s) {
      const unsigned o = offs(s);
#pragma unroll
      for (int u = 0; u < 4; ++u) {
        if (s * 4 + u == BARF) {
          asm volatile("s_barrier" ::: "memory"); SCHED();
        }
        if (s * 4 + u >= IB) asm volatile("s_waitcnt vmcnt(%0)" :: "n"(PD - 1) : "memory");
        if (s == 1 && u == 0) { SCHED(); pt3 = (unsigned)__builtin_amdgcn_s_memrealtime(); SCHED(); }
        kb[s][u] = __builtin_amdgcn_raw_buffer_load_b128(rk, voff + u * 1024, o, KV_AUX);
        SCHED();
        if (s == 0 && u == 0) { pt2 = (unsigned)__builtin_amdgcn_s_memrealtime(); SCHED(); }
      }
    }
    {
      int spin = 0;
#pragma unroll 1
      while (__builtin_amdgcn_readfirstlane(LDL(&ctl[1])) == 0 && ++spin < SPIN_MAX) __builtin_amdgcn_s_sleep(1);
    }
    asm volatile("" ::: "memory");
    const u32x4 q0 = *(const u32x4*)&qs[(lane & 31) * 4], q1 = *(const u32x4*)&qs[128 + (lane & 31) * 4];
    tag = __builtin_amdgcn_readfirstlane(LDL(&ctl[2]));
    if (lane == 0) *(u32x4*)(ws + (8u << 20) + (b * 16 + wave) * 16) = u32x4{pt0, pt1, pt2, pt3};
    float mx = -INFINITY;
    const bool b3 = (lane & 8) != 0, b2 = (lane & 4) != 0;
    unsigned short* scw = &scs[wave * 16 + (lane >> 5) * 8 + ((lane >> 3) & 1) * 2 + (lane & 4) + ((lane >> 4) & 1)];
#pragma unroll
    for (int s = 0; s < NL; ++s) {
      kstep<true>(kb[NBACK], q0, q1, mx, scw + (NBACK + s) * 128, rk, voff, offs(NBACK + 1 + s), b3, b2);
#pragma unroll
      for (int i = s * BPL; i < s * BPL + BPL && i < NBACK; ++i) kstep<false>(kb[i], q0, q1, mx, scw + i * 128, rk, voff, 0, b3, b2);
      SCHED();
    }
    if (SE <= NBACK + NL) arrive_only(&ctl[7]);
#pragma unroll 1
    for (int n = NBACK + NL; n < 16; ++n) {
      const rsrc_t rn = __builtin_amdgcn_make_buffer_rsrc((void*)((n < 15 ? K : V) + (size_t)g * HEADB), 0, HEADB, 0x00020000);
      kstep<true>(kb[NBACK], q0, q1, mx, scw + n * 128, rn, voff, offs(n < 15 ? n + 1 : SE), b3, b2);
      if (n == SE - 1 || n == 15) arrive_only(&ctl[n == 15 ? 8 : 7]);
    }
    mx = __builtin_fmaxf(mx, dpp<XOR7>(mx)); mx = __builtin_fmaxf(mx, dpp<ROR8>(mx));
    mx = rbf(sw32_max(mx)) * CSC;
    float mcl = (lane & 1) ? rdl(mx, 16) : rdl(mx, 0);
    f32x2 a0[4], a1[4];
#pragma unroll
    for (int k = 0; k < 4; ++k) { a0[k] = f32x2{0.f, 0.f}; a1[k] = f32x2{0.f, 0.f}; }
    const int svo = (lane >> 5) * 8 + (lane & 7);
    const unsigned short* scr = &scs[wave * 16 + svo];
    unsigned sraw = scr[SE * 128];
#pragma unroll 1
    for (int n = SE + 1; n < 16; ++n) {
      const unsigned sn = scr[n * 128];
      vstep(kb[NBACK], sraw, mcl, a0, a1, rv, voff, offs(n));
      sraw = sn;
    }
    auto ticket = [&]() __attribute__((always_inline)) -> unsigned {
      asm volatile("s_waitcnt vmcnt(3)" ::: "memory");
      int t = arrive(&ctl[0]);
      asm volatile("s_waitcnt lgkmcnt(0)" : "+v"(t) :: "memory");
      t = __builtin_amdgcn_readfirstlane(t);
      unsigned o = __builtin_amdgcn_readfirstlane(LDL(&uaddr[t]));
      int spin = 0;
#pragma unroll 1
      while (o == EMPTY && ++spin < SPIN_MAX) { __builtin_amdgcn_s_sleep(1); o = __builtin_amdgcn_readfirstlane(LDL(&uaddr[t])); }
      asm volatile("" ::: "memory");
      return o == EMPTY ? OOB : o;
    };
    unsigned o = ticket();
    {
      const float gl = gm[lane & 1];
      const float f = __builtin_amdgcn_exp2f(mcl - gl);
      const float f0 = rdl(f, 0), f1 = rdl(f, 1);
      mcl = gl;
#pragma unroll
      for (int k = 0; k < 4; ++k) { a0[k] *= f0; a1[k] *= f1; }
    }
    const rsrc_t rs = __builtin_amdgcn_make_buffer_rsrc((void*)(ws + WS_SC + g * 131072), 0, 131072, 0x00020000);
#pragma unroll 1
    for (;;) {
      const unsigned sn = __builtin_amdgcn_raw_buffer_load_b16(rs, svo * 2, o >> 7, DEV);
      SCHED();
      vstep(kb[NBACK], sraw, mcl, a0, a1, rv, voff, o);
      if (o == OOB) break;
      sraw = sn;
      o = ticket();
    }
    f32x4 lo, hi;
    lo[0] = sw32_add(a0[0][0], a1[0][0]); lo[1] = sw32_add(a0[0][1], a1[0][1]);
    lo[2] = sw32_add(a0[1][0], a1[1][0]); lo[3] = sw32_add(a0[1][1], a1[1][1]);
    hi[0] = sw32_add(a0[2][0], a1[2][0]); hi[1] = sw32_add(a0[2][1], a1[2][1]);
    hi[2] = sw32_add(a0[3][0], a1[3][0]); hi[3] = sw32_add(a0[3][1], a1[3][1]);
    *(f32x4*)&macc[wave * 512 + lane * 4] = lo;
    *(f32x4*)&macc[wave * 512 + 256 + lane * 4] = hi;
    int arr = arrive(&ctl[3]);
    asm volatile("s_waitcnt lgkmcnt(0)" : "+v"(arr) :: "memory");
    if (__builtin_amdgcn_readfirstlane(arr) == 7) {
      f32x4 pl[7], ph[7];
#pragma unroll
      for (int w = 0; w < 7; ++w) { const int x = ((wave + 1 + w) & 7) * 512 + lane * 4; pl[w] = *(const f32x4*)&macc[x]; ph[w] = *(const f32x4*)&macc[x + 256]; }
      lo += pl[0] + (pl[1] + pl[2]) + ((pl[3] + pl[4]) + (pl[5] + pl[6]));
      hi += ph[0] + (ph[1] + ph[2]) + ((ph[3] + ph[4]) + (ph[5] + ph[6]));
      const float sv[8] = {lo[0], lo[1], lo[2], lo[3], hi[0], hi[1], hi[2], hi[3]};
      const int vo = (lane >> 3) * 16384 + (lane & 7) * 16;
#pragma unroll
      for (int i = 0; i < 4; ++i)
        put(rw, WS_H1 + g * 131072 + i * 4096 + j32 * 128 + (vo), f2u(sv[2 * i]), f2u(sv[2 * i + 1]), tag);
      SCHED();
      if (lane == 0) STL(&ctl[6], 1);
    }
    const int c2 = b / 9;
    if (b % 9 == 0 && c2 < 28) {
      const int ow = (c2 >> 2) * 32 + (lane & 7) * 4 + (c2 & 3);
      const unsigned xv = x32[ow];
      unsigned off[4]; float v0[4], v1[4];
#pragma unroll
      for (int i = 0; i < 4; ++i) { off[i] = WS_H2 + (c2 * 256 + wave * 32) * 128 + i * 1024 + lane * 16; v0[i] = 0.f; v1[i] = 0.f; }
      {
        int spin = 0;
#pragma unroll 1
        while (__builtin_amdgcn_readfirstlane(LDL(&ctl[6])) == 0 && ++spin < SPIN_MAX) __builtin_amdgcn_s_sleep(1);
        asm volatile("" ::: "memory");
        u32x4 t[4];
        poll_issue<4>(rw, off, t);
        poll_check<4>(t, off, tag, v0, v1);
#pragma unroll 1
        while (__builtin_amdgcn_readfirstlane(LDL(&ctl[5])) == 0 && ++spin < SPIN_MAX) __builtin_amdgcn_s_sleep(1);
        asm volatile("" ::: "memory");
      }
      gather<4>(rw, off, tag, v0, v1);
      float s = sw32_add(v0[0] + v0[1] + (v0[2] + v0[3]), v1[0] + v1[1] + (v1[2] + v1[3]));
      s += dpp<ROR8>(s); s = sw16_add(s, s);
      if ((lane & 24) == 0) red[wave * 16 + (lane & 7) * 2 + (lane >> 5)] = s;
      int fin = arrive(&ctl[4]);
      const f32x2 r = *(const f32x2*)&red[lane * 2];
      asm volatile("s_waitcnt lgkmcnt(0)" : "+v"(fin) :: "memory");
      if (__builtin_amdgcn_readfirstlane(fin) == 7) {
        float r0 = r[0], r1 = r[1];
        r0 += dpp<ROR8>(r0); r1 += dpp<ROR8>(r1);
        r0 = sw16_add(r0, r0); r1 = sw16_add(r1, r1);
        r0 = sw32_add(r0, r0); r1 = sw32_add(r1, r1);
        if (lane < 8) {
          const unsigned ov = pkbf(r0 + u2f(xv << 16), r1 + u2f(xv & 0xffff0000u));
          __builtin_amdgcn_raw_buffer_store_b32(ov, __builtin_amdgcn_make_buffer_rsrc((void*)out32, 0, 896, 0x00020000), ow * 4, 0, DEV);
        }
        if (b == 0 && lane == 0) __builtin_amdgcn_raw_buffer_store_b32(tag, rw, 0, WS_EPOCH, DEV);
      }
    }
    asm volatile("" :: "v"(kb[NBACK][0]), "v"(kb[NBACK][1]), "v"(kb[NBACK][2]), "v"(kb[NBACK][3]));
  } else {
    __builtin_amdgcn_s_setprio(3);
    const unsigned ep = __builtin_amdgcn_raw_buffer_load_b32(rw, 0, WS_EPOCH, DEV);
    SCHED(); pt2 = (unsigned)__builtin_amdgcn_s_memrealtime(); SCHED();
    unsigned xr[7];
#pragma unroll
    for (int j = 0; j < 7; ++j) xr[j] = x32[(lane >> 1) + 32 * j];
    u32x4 w[14];
    const int jq = mem * 4 + (grp & 3);
    {
      const rsrc_t r = __builtin_amdgcn_make_buffer_rsrc((void*)wq, 0, 448 * 8192, 0x00020000);
      const int vo = (lane >> 1) * 16384 + (lane & 1) * 16;
#pragma unroll
      for (int i = 0; i < 14; ++i) w[i] = __builtin_amdgcn_raw_buffer_load_b128(r, vo, g * 1024 + jq * 32 + (i >> 1) * 524288 + (i & 1) * 8192, KV_AUX);
    }
    if (j32 == 0 && lane == 0) __builtin_amdgcn_raw_buffer_store_b32(0u, rw, 0, WS_CTR + g * 256, DEV);
    SCHED();
    *(u32x4*)&uaddr[lane * 4] = u32x4{EMPTY, EMPTY, EMPTY, EMPTY};
    *(u32x4*)&uaddr[256 + lane * 4] = u32x4{EMPTY, EMPTY, EMPTY, EMPTY};
    if (lane < 16) ctl[lane] = 0;
    if (ABAR == 0) asm volatile("s_waitcnt lgkmcnt(0)\n s_barrier" ::: "memory");
    asm volatile("" : "+v"(xr[0]), "+v"(xr[1]), "+v"(xr[2]), "+v"(xr[3]), "+v"(xr[4]), "+v"(xr[5]), "+v"(xr[6]));
    SCHED(); pt3 = (unsigned)__builtin_amdgcn_s_memrealtime(); SCHED();
    unsigned xp[7];
    {
      float s = 0.f;
#pragma unroll
      for (int j = 0; j < 7; ++j) s += u2f(xr[j] << 16) + u2f(xr[j] & 0xffff0000u);
      const float mean = wave_sum(s) * (1.f / 896.f);
      float v = 0.f;
#pragma unroll
      for (int j = 0; j < 7; ++j) { const float da = u2f(xr[j] << 16) - mean, db = u2f(xr[j] & 0xffff0000u) - mean; v += da * da + db * db; }
      const float var = wave_sum(v) * (1.f / 896.f);
      const float mb = rbf(mean), rden = __builtin_amdgcn_rcpf(rbf(__builtin_amdgcn_sqrtf(rbf(rbf(var) + 1e-5f))));
#pragma unroll
      for (int j = 0; j < 7; ++j) xp[j] = pkbf(rbf(u2f(xr[j] << 16) - mb) * rden, rbf(u2f(xr[j] & 0xffff0000u) - mb) * rden);
    }
    tag = __builtin_amdgcn_readfirstlane(ep) + 1u;
    {
      float r0, r1;
      gemv16(w, xp, r0, r1);
      const unsigned pk = pkbf(r0, r1);
      auto s = __builtin_amdgcn_permlane16_swap(pk, pk, false, false);
      unsigned s0 = s[0], s1 = s[1];
      asm volatile("s_waitcnt vmcnt(0)" : "+v"(s0), "+v"(s1) :: "memory");
      if ((lane & 30) == 0)
        put(rw, WS_Q + g * 2048 + jq * 64 + (((lane & 1) * 2 + (lane >> 5)) * 16), s0, s1, tag);
    }
#pragma unroll
    for (int j = 0; j < 7; ++j) xs[(lane >> 1) + 32 * j] = xp[j];
    if (ABAR == 1) asm volatile("s_waitcnt lgkmcnt(0)\n s_barrier" ::: "memory");
    {
      unsigned off[2] = {WS_Q + g * 2048 + lane * 16u, WS_Q + g * 2048 + 1024 + lane * 16u};
      float v0[2] = {0.f, 0.f}, v1[2] = {0.f, 0.f};
      gather<2>(rw, off, tag, v0, v1);
      *(f32x2*)&qs[lane * 2] = f32x2{v0[0], v1[0]};
      *(f32x2*)&qs[128 + lane * 2] = f32x2{v0[1], v1[1]};
      if (lane == 0) STL(&ctl[2], (int)tag);
      asm volatile("s_waitcnt lgkmcnt(0)" ::: "memory");
      if (lane == 0) STL(&ctl[1], 1);
    }
    if (lane == 0) *(u32x4*)(ws + (8u << 20) + (b * 16 + wave) * 16) = u32x4{pt0, pt1, pt2, pt3};
    const int fu = grp * 7 + mem;
    if (mem < 7) {
      const rsrc_t rg = __builtin_amdgcn_make_buffer_rsrc((void*)wg, 0, 448 * 3584, 0x00020000);
      const rsrc_t ru = __builtin_amdgcn_make_buffer_rsrc((void*)wu, 0, 448 * 3584, 0x00020000);
      const int vo = (lane >> 3) * 3584 + (lane & 7) * 16, so = (fu & 7) * 56 * 3584 + (fu >> 3) * 128;
#pragma unroll
      for (int i = 0; i < 7; ++i) {
        w[2 * i] = __builtin_amdgcn_raw_buffer_load_b128(rg, vo, so + i * 8 * 3584, KV_AUX);
        w[2 * i + 1] = __builtin_amdgcn_raw_buffer_load_b128(ru, vo, so + i * 8 * 3584, KV_AUX);
      }
      float a[16];
#pragma unroll
      for (int i = 0; i < 16; ++i) a[i] = 0.f;
#pragma unroll
      for (int i = 0; i < 7; ++i) {
        const unsigned xl = ((const unsigned short*)xs)[(fu & 7) * 56 + i * 8 + (lane >> 3)], xh = xl << 16;
#pragma unroll
        for (int e = 0; e < 4; ++e) {
          const unsigned wa = w[2 * i][e], wb = w[2 * i + 1][e];
          a[2 * e] = dot2(wa, xl, a[2 * e]); a[2 * e + 1] = dot2(wa, xh, a[2 * e + 1]);
          a[8 + 2 * e] = dot2(wb, xl, a[8 + 2 * e]); a[8 + 2 * e + 1] = dot2(wb, xh, a[8 + 2 * e + 1]);
        }
      }
#pragma unroll
      for (int i = 0; i < 8; ++i) a[i] = sw32_add(a[i], a[i + 8]);
#pragma unroll
      for (int i = 0; i < 4; ++i) { a[i] = sw16_add(a[i], a[i + 4]); a[i] += dpp<ROR8>(a[i]); }
      const bool b3 = (lane & 8) != 0;
      const int src = (((lane & 7) << 3) | (lane >> 3)) * 4;
      const unsigned d0 = __builtin_amdgcn_ds_bpermute(src, f2u(b3 ? a[2] : a[0])), d1 = __builtin_amdgcn_ds_bpermute(src, f2u(b3 ? a[3] : a[1]));
      put(rw, WS_F + (fu >> 3) * 8192 + (fu & 7) * 128 + ((lane >> 3) * 1024 + (lane & 7) * 16), d0, d1, tag);
    }
    SCHED();
    float fo[8];
    unsigned wp[8][8], hp = 0;
    const rsrc_t ro = __builtin_amdgcn_make_buffer_rsrc((void*)wo, 0, 4096 * 896, 0x00020000);
    const rsrc_t rd = __builtin_amdgcn_make_buffer_rsrc((void*)wd, 0, 1792 * 896, 0x00020000);
    const int row0 = g * 512 + (j32 >> 2) * 64 + (j32 & 3) * 2;
    const unsigned wvo = lane < 56 ? lane * 16u : OOB;
    u32x4 ov[16], dv[8];
    auto issue_w = [&]() __attribute__((always_inline)) {
#pragma unroll
      for (int i = 0; i < 8; ++i) dv[i] = __builtin_amdgcn_raw_buffer_load_b128(rd, wvo, mem < 7 ? (fu * 8 + i) * 896 : OOB, KV_AUX);
#pragma unroll
      for (int i = 0; i < 16; ++i) ov[i] = __builtin_amdgcn_raw_buffer_load_b128(ro, wvo, (row0 + (i >> 1) * 8 + (i & 1)) * 896, KV_AUX);
      SCHED();
    };
    auto prep_d = [&]() __attribute__((always_inline)) {
#pragma unroll
      for (int i = 0; i < 8; ++i) fo[i] = 0.f;
#pragma unroll
      for (int pc = 0; pc < 4; ++pc) {
        const unsigned hs = __builtin_amdgcn_readlane(hp, pc);
#pragma unroll
        for (int k = 0; k < 4; ++k) {
          unsigned w0 = dv[2 * pc][k], w1 = dv[2 * pc + 1][k];
          fo[2 * k] = dot2(__builtin_amdgcn_perm(w1, w0, 0x05040100u), hs, fo[2 * k]);
          fo[2 * k + 1] = dot2(__builtin_amdgcn_perm(w1, w0, 0x07060302u), hs, fo[2 * k + 1]);
        }
      }
      asm volatile("" : "+v"(fo[0]), "+v"(fo[1]), "+v"(fo[2]), "+v"(fo[3]), "+v"(fo[4]), "+v"(fo[5]), "+v"(fo[6]), "+v"(fo[7]));
      SCHED();
    };
    auto prep_o = [&]() __attribute__((always_inline)) {
#pragma unroll
      for (int pc = 0; pc < 8; ++pc)
#pragma unroll
        for (int k = 0; k < 4; ++k) {
          unsigned w0 = ov[2 * pc][k], w1 = ov[2 * pc + 1][k];
          wp[pc][2 * k] = __builtin_amdgcn_perm(w1, w0, 0x05040100u);
          wp[pc][2 * k + 1] = __builtin_amdgcn_perm(w1, w0, 0x07060302u);
          asm volatile("" : "+v"(wp[pc][2 * k]), "+v"(wp[pc][2 * k + 1]));
        }
      SCHED();
    };
    {
      const unsigned t0 = __builtin_amdgcn_raw_buffer_load_b32(ro, 0, row0 * 896, KV_AUX), t1 = __builtin_amdgcn_raw_buffer_load_b32(rd, 0, fu * 8 * 896, KV_AUX);
      asm volatile("" :: "v"(t0), "v"(t1));
    }
    if (mem < 7) {
      unsigned off[1] = {WS_F + fu * 1024 + lane * 16u};
      float v0[1] = {0.f}, v1[1] = {0.f};
      gather<1>(rw, off, tag, v0, v1);
      float r0 = v0[0], r1 = v1[0];
      r0 += dpp<ROR8>(r0); r1 += dpp<ROR8>(r1);
      r0 = sw16_add(r0, r0); r1 = sw16_add(r1, r1);
      r0 = rbf(sw32_add(r0, r0)); r1 = rbf(sw32_add(r1, r1));
      const float u0 = dpp<ROR4>(r0), u1 = dpp<ROR4>(r1);
      const float s0 = rbf(r0 * __builtin_amdgcn_rcpf(1.f + __expf(-r0))), s1 = rbf(r1 * __builtin_amdgcn_rcpf(1.f + __expf(-r1)));
      hp = pkbf(s0 * u0, s1 * u1);
    }
    {
      int spin = 0;
#pragma unroll 1
      while (__builtin_amdgcn_readfirstlane(LDL(&ctl[7])) < 8 && ++spin < SPIN_MAX) __builtin_amdgcn_s_sleep(2);
      asm volatile("" ::: "memory");
      constexpr int NQ = (SE + 3) / 4;
      u32x4 sq[4];
      float m0 = -INFINITY, m1 = -INFINITY;
#pragma unroll
      for (int i = 0; i < NQ; ++i) {
        const int st = i * 4 + (lane >> 4) < SE ? i * 4 + (lane >> 4) : 0;
        sq[i] = *(const u32x4*)&scs[st * 128 + (lane & 15) * 8];
        __builtin_amdgcn_raw_buffer_store_b128(sq[i], rw, WS_SC + g * 131072 + (wbase >> 7) + (((st + RG * g) & 15) << 13) + (lane & 15) * 16, 0, DEV);
      }
      SCHED();
#pragma unroll
      for (int i = 0; i < 4 * NQ; ++i) { const unsigned w = sq[i >> 2][i & 3]; m0 = __builtin_fmaxf(m0, u2f(w << 16)); m1 = __builtin_fmaxf(m1, u2f(w & 0xffff0000u)); }
      m0 = wave_max(m0); m1 = wave_max(m1);
      asm volatile("s_waitcnt vmcnt(0)" : "+v"(m0), "+v"(m1) :: "memory");
      if (lane == 0) put(rw, WS_ML + g * 2048 + 1024 + j32 * 16, f2u(m0), f2u(m1), tag);
      unsigned off[1] = {WS_ML + g * 2048 + 1024 + (lane & 31) * 16u};
      float v0[1] = {0.f}, v1[1] = {0.f};
#pragma unroll 1
      while (__builtin_amdgcn_readfirstlane(LDL(&ctl[8])) < 8 && ++spin < SPIN_MAX) __builtin_amdgcn_s_sleep(2);
      asm volatile("" ::: "memory");
      gather<1, 4>(rw, off, tag, v0, v1);
      m0 = v0[0]; m1 = v1[0];
#pragma unroll
      for (int i = 0; i < 4; ++i) sq[i] = *(const u32x4*)&scs[i * 512 + lane * 8];
#pragma unroll
      for (int i = 0; i < 16; ++i) { const unsigned w = sq[i >> 2][i & 3]; m0 = __builtin_fmaxf(m0, u2f(w << 16)); m1 = __builtin_fmaxf(m1, u2f(w & 0xffff0000u)); }
      m0 = wave_max(m0) * CSC; m1 = wave_max(m1) * CSC;
      if (lane < 2) gm[lane] = lane ? m1 : m0;
      asm volatile("s_waitcnt lgkmcnt(0)" ::: "memory");
      if (lane < NPRE) STL(&uaddr[lane], (int)(wbase + (lane & 7) * 4096 + (((SE - TP + (lane >> 3) + RG * g) & 15) << 20)));
      float l0 = 0.f, l1 = 0.f;
#pragma unroll
      for (int i = 0; i < 16; ++i) {
        const unsigned w = sq[i >> 2][i & 3];
        l0 += __builtin_amdgcn_exp2f(__builtin_fmaf(u2f(w << 16), CSC, -m0)); l1 += __builtin_amdgcn_exp2f(__builtin_fmaf(u2f(w & 0xffff0000u), CSC, -m1));
      }
      l0 = wave_sum(l0); l1 = wave_sum(l1);
      if ((lane & 31) == 0)
        put(rw, WS_ML + g * 2048 + j32 * 16 + ((lane >> 5) * 512), f2u(lane ? m1 : m0), f2u(lane ? l1 : l0), tag);
    }
    {
      const rsrc_t rc = __builtin_amdgcn_make_buffer_rsrc((void*)(ws + WS_CTR + g * 256), 0, 4, 0x00020000);
      int k = NPRE, want = G1;
#pragma unroll 1
      for (int spin = 0; spin < 16 * SPIN_MAX; ++spin) {
        const int t = __builtin_amdgcn_readfirstlane(LDL(&ctl[0]));
        if (k - t <= THRESH) {
          const int gr = want > G1 ? G1 : k - t <= 0 && want < GMIN ? GMIN : want;
          const unsigned p = (unsigned)__builtin_amdgcn_readfirstlane(__builtin_amdgcn_raw_ptr_buffer_atomic_add_i32(gr, rc, lane * 4, 0, DEV));
          const int rem = NP - (int)p - gr;
          const bool last = rem <= 0 || k + gr + G1 > UCAP;
          const unsigned pu = p + lane;
          const unsigned o = (pu * 4096u + ((RG * g) << 20)) & (HEADB - 1);
          const bool valid = pu < (unsigned)NP && lane < gr;
          if (valid || last) STL(&uaddr[k + lane], (int)(valid ? o : OOB));
          if (last) break;
          k += gr;
          const int t2 = __builtin_amdgcn_readfirstlane(LDL(&ctl[0]));
          const float ta = 0.5f * (float)(t + t2);
          const float rho = (S_TICK * 8 + ta) * __builtin_amdgcn_rcpf(256.f - Q0 - rem * (1.f / 32.f));
          const int kfin = __builtin_amdgcn_readfirstlane((int)(rho * (rem * (1.f / 32.f) + Q0 + FL) - FL + ta + 0.5f));
          want = kfin - k < 0 ? 0 : kfin - k;
        } else {
          __builtin_amdgcn_s_sleep(1);
        }
      }
    }
    {
      unsigned off[4]; float v0[4], v1[4];
#pragma unroll
      for (int i = 0; i < 4; ++i) { off[i] = WS_H1 + b * 4096 + i * 1024 + lane * 16; v0[i] = 0.f; v1[i] = 0.f; }
      int spin = 0;
#pragma unroll 1
      while (__builtin_amdgcn_readfirstlane(LDL(&ctl[3])) < LATEW && ++spin < SPIN_MAX) __builtin_amdgcn_s_sleep(1);
      asm volatile("" ::: "memory");
      float f0, f1, f2, f3;
      {
        unsigned fo[1] = {WS_ML + g * 2048 + (j32 >> 4) * 512 + ((lane >> 3) + 8 * (lane & 3)) * 16u};
        float m[1] = {0.f}, l[1] = {0.f};
        u32x4 t[1];
        poll_issue<1>(rw, fo, t);
        issue_w();
        if (!poll_check<1>(t, fo, tag, m, l)) gather<1>(rw, fo, tag, m, l);
        float M = m[0];
        M = __builtin_fmaxf(M, dpp<XOR1>(M)); M = __builtin_fmaxf(M, dpp<XOR2>(M)); M = __builtin_fmaxf(M, dpp<ROR8>(M));
        M = sw16_max(M); M = sw32_max(M);
        float f = __builtin_amdgcn_exp2f(m[0] - M);
        float L = f * l[0];
        L += dpp<XOR1>(L); L += dpp<XOR2>(L); L += dpp<ROR8>(L); L = sw16_add(L, L); L = sw32_add(L, L);
        f *= __builtin_amdgcn_rcpf(L);
        f0 = dpp<0x00>(f); f1 = dpp<0x55>(f); f2 = dpp<0xAA>(f); f3 = dpp<0xFF>(f);
        asm volatile("" : "+v"(f0), "+v"(f1), "+v"(f2), "+v"(f3));
      }
      prep_d();
      prep_o();
#pragma unroll 1
      while (__builtin_amdgcn_readfirstlane(LDL(&ctl[6])) == 0 && ++spin < SPIN_MAX) __builtin_amdgcn_s_sleep(1);
      asm volatile("" ::: "memory");
      gather<4>(rw, off, tag, v0, v1);
      float s0 = f0 * v0[0] + f1 * v0[1] + (f2 * v0[2] + f3 * v0[3]), s1 = f0 * v1[0] + f1 * v1[1] + (f2 * v1[2] + f3 * v1[3]);
      s0 += dpp<ROR8>(s0); s1 += dpp<ROR8>(s1);
      s0 = sw16_add(s0, s0); s1 = sw16_add(s1, s1);
      s0 = sw32_add(s0, s0); s1 = sw32_add(s1, s1);
      const unsigned op = pkbf(s0, s1);
#pragma unroll
      for (int pc = 0; pc < 8; ++pc) {
        const unsigned os = __builtin_amdgcn_readlane(op, pc);
#pragma unroll
        for (int c = 0; c < 8; ++c) fo[c] = dot2(wp[pc][c], os, fo[c]);
      }
      if (lane < 56) {
        const int vo = (lane >> 3) * (4 * 256 * 128) + (lane & 7) * 16;
#pragma unroll
        for (int i = 0; i < 4; ++i)
          put(rw, WS_H2 + (i * 256 + b) * 128 + (vo), f2u(fo[2 * i]), f2u(fo[2 * i + 1]), tag);
      }
      SCHED();
      if (lane == 0) STL(&ctl[5], 1);
    }
  }
}

__global__ void report(char* ws) {
  const unsigned ts = (unsigned)__builtin_amdgcn_s_memrealtime();
  const u32x4* h = (const u32x4*)(ws + (8u << 20));
  __shared__ int r, mn;
  const int t = threadIdx.x;
  const unsigned base = h[8][0];
  if (t == 0) { r = 0; mn = 1 << 20; }
  __syncthreads();
  { int m = 1 << 20; for (int w = 0; w < 9; ++w) { const int d = (int)(h[t * 16 + w][0] - base); m = d < m ? d : m; } atomicMin(&mn, m); }
  __syncthreads();
  const unsigned tmin = base + mn;
  { int acc = 0; for (int w = 0; w < 8; ++w) { const u32x4 q = h[t * 16 + w]; const unsigned t0 = q[0], t1 = q[1], t2 = q[2], t3 = q[3]; acc += (int)(t2 - t0); } atomicAdd(&r, acc); }
  __syncthreads();
  if (t == 0) {
    int v = r * 40 / (256 * 8); v = v < 0 ? 0 : v > 20000 ? 20000 : v;
    const unsigned until = ts + v + 5000u;
    for (int i = 0; i < 1000000; ++i) { if ((int)((unsigned)__builtin_amdgcn_s_memrealtime() - until) >= 0) break; __builtin_amdgcn_s_sleep(1); }
  }
}
extern "C" void kernel_launch(void* const* d_in, const int* in_sizes, int n_in, void* d_out, int out_size, void* d_ws,
                              size_t ws_size, hipStream_t stream) {
  fused_decode<<<256, 576, 0, stream>>>((const unsigned*)d_in[0], (const char*)d_in[1], (const char*)d_in[2],
                                        (const char*)d_in[3], (const char*)d_in[4], (const char*)d_in[6],
                                        (const char*)d_in[7], (const char*)d_in[8], (unsigned*)d_out, (char*)d_ws);
  report<<<1, 256, 0, stream>>>((char*)d_ws);
}
```

```cpp
#include <hip/hip_runtime.h>
#include <stdint.h>

typedef unsigned u32x4 __attribute__((ext_vector_type(4)));
typedef unsigned u32x2 __attribute__((ext_vector_type(2)));
typedef float f32x2 __attribute__((ext_vector_type(2)));
typedef float f32x4 __attribute__((ext_vector_type(4)));
typedef __bf16 bf16x2 __attribute__((ext_vector_type(2)));
#define LAS __attribute__((address_space(3)))
typedef __amdgpu_buffer_rsrc_t rsrc_t;

#define SV 6
#define TP 2
#define S_TICK (16 + SV)
#define S_STATIC (S_TICK + TP)
#define NBACK 6
#define BPL 2
#define RG 2
#define THRESH 8
#define G1 12
#define GMIN 2
#define Q0 4.f
#define FL 8.f
#define LATEW 1
#define IB 4
#define PD 4
#define BARF 16
#define ABAR 1
#define KV_AUX 2
#define DEV 16

constexpr unsigned HEADB = 16u << 20;
constexpr int NP = (32 - S_STATIC) * 256;
constexpr int NPRE = (S_STATIC - S_TICK) * 8;
constexpr int SE = 16 - SV;
constexpr int NL = (NBACK + BPL - 1) / BPL;
static_assert(NPRE <= 64 && SV >= 1 && NBACK + NL < 16, "");
constexpr int UCAP = 448;
static_assert(NP / (UCAP - NPRE - 2 * G1) <= 31, "someone stays until the pool is empty");
constexpr unsigned OOB = 0x40000000u, EMPTY = 0xffffffffu;
constexpr int SPIN_MAX = 200000;
constexpr unsigned WS_EPOCH = 0, WS_CTR = 4096, WS_Q = 16384, WS_ML = 32768, WS_H1 = 65536, WS_H2 = WS_H1 + (1u << 20);
constexpr unsigned WS_F = 2u << 20, WS_SC = 3u << 20, WS_USED = 4u << 20;
constexpr float CSC = 0.0625f * 1.4426950408889634f;

__device__ __forceinline__ float u2f(unsigned u) { return __builtin_bit_cast(float, u); }
__device__ __forceinline__ unsigned f2u(float f) { return __builtin_bit_cast(unsigned, f); }
__device__ __forceinline__ float rbf(float f) { __bf16 b = (__bf16)f; return (float)b; }
__device__ __forceinline__ unsigned pkbf(float lo, float hi) {
  bf16x2 v; v[0] = (__bf16)lo; v[1] = (__bf16)hi; return __builtin_bit_cast(unsigned, v);
}
__device__ __forceinline__ float dot2(unsigned a, unsigned b, float acc) {
  return __builtin_amdgcn_fdot2_f32_bf16(__builtin_bit_cast(bf16x2, a), __builtin_bit_cast(bf16x2, b), acc, false);
}
template <int CTRL> __device__ __forceinline__ float dpp(float x) {
  return __builtin_bit_cast(float, __builtin_amdgcn_mov_dpp(__builtin_bit_cast(int, x), CTRL, 0xf, 0xf, true));
}
constexpr int XOR1 = 0xB1, XOR2 = 0x4E, XOR7 = 0x141, ROR8 = 0x128, ROR4 = 0x124, BC0 = 0x150, BC8 = 0x158;
__device__ __forceinline__ float sw16_add(float a, float b) {
  auto s = __builtin_amdgcn_permlane16_swap(f2u(a), f2u(b), false, false);
  unsigned s0 = s[0], s1 = s[1]; return u2f(s0) + u2f(s1);
}
__device__ __forceinline__ float sw32_add(float a, float b) {
  auto s = __builtin_amdgcn_permlane32_swap(f2u(a), f2u(b), false, false);
  unsigned s0 = s[0], s1 = s[1]; return u2f(s0) + u2f(s1);
}
__device__ __forceinline__ float sw16_max(float a) {
  auto s = __builtin_amdgcn_permlane16_swap(f2u(a), f2u(a), false, false);
  unsigned s0 = s[0], s1 = s[1]; return __builtin_fmaxf(u2f(s0), u2f(s1));
}
__device__ __forceinline__ float sw32_max(float a) {
  auto s = __builtin_amdgcn_permlane32_swap(f2u(a), f2u(a), false, false);
  unsigned s0 = s[0], s1 = s[1]; return __builtin_fmaxf(u2f(s0), u2f(s1));
}
__device__ __forceinline__ float wave_sum(float t) {
  t += dpp<XOR1>(t); t += dpp<XOR2>(t); t += dpp<XOR7>(t); t += dpp<ROR8>(t);
  t = sw16_add(t, t); return sw32_add(t, t);
}
__device__ __forceinline__ float wave_max(float t) {
  t = __builtin_fmaxf(t, dpp<XOR1>(t)); t = __builtin_fmaxf(t, dpp<XOR2>(t)); t = __builtin_fmaxf(t, dpp<XOR7>(t));
  t = __builtin_fmaxf(t, dpp<ROR8>(t)); return sw32_max(sw16_max(t));
}
__device__ __forceinline__ float rdl(float v, int l) { return u2f(__builtin_amdgcn_readlane(f2u(v), l)); }
#define LDL(p) __hip_atomic_load((LAS int*)(p), __ATOMIC_RELAXED, __HIP_MEMORY_SCOPE_WORKGROUP)
#define STL(p, v) __hip_atomic_store((LAS int*)(p), v, __ATOMIC_RELAXED, __HIP_MEMORY_SCOPE_WORKGROUP)
#define SCHED() __builtin_amdgcn_sched_barrier(0)

template <bool REFILL>
__device__ __forceinline__ void kstep(u32x4 (&r)[4], const u32x4& q0, const u32x4& q1, float& mx, unsigned short* sc,
                                      rsrc_t rn, int voff, unsigned o, bool b3, bool b2) {
  float e[4];
#pragma unroll
  for (int u = 0; u < 4; ++u) {
    const u32x4 k = r[u];
    float x = 0.f, y = 0.f;
#pragma unroll
    for (int i = 0; i < 4; ++i) { unsigned w = k[i], qa = q0[i], qb = q1[i]; x = dot2(w, qa, x); y = dot2(w, qb, y); }
    if (REFILL) {
      asm volatile("" : "+v"(x), "+v"(y) :: "memory");
      r[u] = __builtin_amdgcn_raw_buffer_load_b128(rn, voff + u * 1024, o, KV_AUX);
      SCHED();
    }
    e[u] = sw16_add(x, y);
    e[u] += dpp<ROR8>(e[u]);
  }
  float s0 = b3 ? e[1] : e[0], s1 = b3 ? e[3] : e[2];
  s0 += dpp<XOR7>(s0); s1 += dpp<XOR7>(s1);
  float t = b2 ? s1 : s0;
  t += dpp<XOR2>(t); t += dpp<XOR1>(t);
  mx = __builtin_fmaxf(mx, t);
  *sc = __builtin_bit_cast(unsigned short, (__bf16)t);
}

__device__ __forceinline__ void vstep(u32x4 (&r)[4], unsigned sraw, float mcl, f32x2 (&a0)[4], f32x2 (&a1)[4], rsrc_t rv,
                                      int voff, unsigned o) {
  const float p = __builtin_amdgcn_exp2f(__builtin_fmaf(u2f(sraw << 16), CSC, -mcl));
  const float pp[8] = {dpp<BC0>(p), dpp<BC0 + 1>(p), dpp<BC0 + 2>(p), dpp<BC0 + 3>(p), dpp<BC0 + 4>(p), dpp<BC0 + 5>(p), dpp<BC0 + 6>(p), dpp<BC0 + 7>(p)};
#pragma unroll
  for (int u = 0; u < 4; ++u) {
    const u32x4 v = r[u];
    const f32x2 p0 = {pp[2 * u], pp[2 * u]}, p1 = {pp[2 * u + 1], pp[2 * u + 1]};
#pragma unroll
    for (int e = 0; e < 4; ++e) {
      unsigned w = v[e];
      const f32x2 x = {u2f(w << 16), u2f(w & 0xffff0000u)};
      a0[e] = __builtin_elementwise_fma(x, p0, a0[e]);
      a1[e] = __builtin_elementwise_fma(x, p1, a1[e]);
    }
    asm volatile("" : "+v"(a0[0]), "+v"(a0[1]), "+v"(a0[2]), "+v"(a0[3]), "+v"(a1[0]), "+v"(a1[1]), "+v"(a1[2]), "+v"(a1[3]) :: "memory");
    r[u] = __builtin_amdgcn_raw_buffer_load_b128(rv, voff + u * 1024, o, KV_AUX);
    SCHED();
  }
}

__device__ __forceinline__ void gemv16(const u32x4 (&w)[14], const unsigned (&xp)[7], float& r0, float& r1) {
  float a[8];
#pragma unroll
  for (int i = 0; i < 8; ++i) a[i] = 0.f;
#pragma unroll
  for (int j = 0; j < 7; ++j) {
#pragma unroll
    for (int k = 0; k < 4; ++k) {
      unsigned w0 = w[2 * j][k], w1 = w[2 * j + 1][k];
      a[2 * k] = dot2(__builtin_amdgcn_perm(w1, w0, 0x05040100u), xp[j], a[2 * k]);
      a[2 * k + 1] = dot2(__builtin_amdgcn_perm(w1, w0, 0x07060302u), xp[j], a[2 * k + 1]);
    }
  }
  float b[4];
#pragma unroll
  for (int i = 0; i < 4; ++i) b[i] = sw32_add(a[i], a[i + 4]);
  r0 = sw16_add(b[0], b[2]); r1 = sw16_add(b[1], b[3]);
  r0 += dpp<ROR8>(r0); r1 += dpp<ROR8>(r1);
  r0 += dpp<ROR4>(r0); r1 += dpp<ROR4>(r1);
  r0 += dpp<XOR2>(r0); r1 += dpp<XOR2>(r1);
}

__device__ __forceinline__ void put(rsrc_t rw, unsigned off, unsigned a, unsigned b, unsigned tag) {
  __builtin_amdgcn_raw_buffer_store_b128(u32x4{a, tag, b, tag}, rw, off, 0, DEV);
}

template <int N> __device__ __forceinline__ void poll_issue(rsrc_t rw, const unsigned (&off)[N], u32x4 (&t)[N]) {
#pragma unroll
  for (int i = 0; i < N; ++i) t[i] = __builtin_amdgcn_raw_buffer_load_b128(rw, off[i], 0, DEV);
  SCHED();
}
template <int N>
__device__ __forceinline__ bool poll_check(const u32x4 (&t)[N], unsigned (&off)[N], unsigned tag, float (&v0)[N], float (&v1)[N]) {
  bool left = false;
#pragma unroll
  for (int i = 0; i < N; ++i) {
    const unsigned t0 = t[i][0], t1 = t[i][1], t2 = t[i][2], t3 = t[i][3];
    const bool ok = (t1 == tag) & (t3 == tag) & (off[i] != OOB);
    v0[i] = ok ? u2f(t0) : v0[i]; v1[i] = ok ? u2f(t2) : v1[i];
    off[i] = ok ? OOB : off[i];
    left |= off[i] != OOB;
  }
  return __builtin_amdgcn_ballot_w64(left) == 0;
}
template <int N, int SL = 1>
__device__ __forceinline__ void gather(rsrc_t rw, unsigned (&off)[N], unsigned tag, float (&v0)[N], float (&v1)[N]) {
#pragma unroll 1
  for (int spin = 0; spin < SPIN_MAX; ++spin) {
    u32x4 t[N];
    poll_issue<N>(rw, off, t);
    if (poll_check<N>(t, off, tag, v0, v1)) break;
    __builtin_amdgcn_s_sleep(SL);
  }
}
__device__ __forceinline__ int arrive(int* p) {
  int r; unsigned long long e;
  asm volatile("s_mov_b64 %1, exec\n s_mov_b64 exec, 1\n ds_add_rtn_u32 %0, %2, %3\n s_mov_b64 exec, %1"
               : "=&v"(r), "=&s"(e) : "v"((unsigned)(uintptr_t)(LAS int*)p), "v"(1) : "memory");
  return r;
}

__device__ __forceinline__ void arrive_only(int* p) {
  unsigned long long e;
  asm volatile("s_mov_b64 %0, exec\n s_mov_b64 exec, 1\n ds_add_u32 %1, %2\n s_mov_b64 exec, %0"
               : "=&s"(e) : "v"((unsigned)(uintptr_t)(LAS int*)p), "v"(1) : "memory");
}

__global__ void __launch_bounds__(576) fused_decode(const unsigned* __restrict__ x32, const char* __restrict__ K,
                                                    const char* __restrict__ V, const char* __restrict__ wq,
                                                    const char* __restrict__ wo, const char* __restrict__ wg,
                                                    const char* __restrict__ wu, const char* __restrict__ wd,
                                                    unsigned* out32, char* ws) {
  __shared__ __attribute__((aligned(16))) float macc[8 * 512];
  __shared__ __attribute__((aligned(16))) unsigned short scs[2048];
  __shared__ __attribute__((aligned(16))) unsigned qs[256];
  __shared__ __attribute__((aligned(16))) unsigned xs[224];
  __shared__ __attribute__((aligned(16))) float gm[2];
  __shared__ __attribute__((aligned(16))) float red[8 * 16];
  __shared__ __attribute__((aligned(16))) unsigned uaddr[512];
  __shared__ int ctl[16];
  const int lane = threadIdx.x & 63, hwave = __builtin_amdgcn_readfirstlane(threadIdx.x >> 6);
  const int wave = hwave == 0 ? 8 : hwave - 1;
  const int b = blockIdx.x, g = b >> 5, grp = b >> 3, mem = b & 7, j32 = b & 31;
  asm volatile("" :: "s"(x32), "s"(K), "s"(V), "s"(wq), "s"(wo), "s"(wg), "s"(wu), "s"(wd), "s"(out32), "s"(ws));
  const rsrc_t rw = __builtin_amdgcn_make_buffer_rsrc((void*)ws, 0, WS_USED, 0x00020000);
  const rsrc_t rk = __builtin_amdgcn_make_buffer_rsrc((void*)(K + (size_t)g * HEADB), 0, HEADB, 0x00020000);
  const rsrc_t rv = __builtin_amdgcn_make_buffer_rsrc((void*)(V + (size_t)g * HEADB), 0, HEADB, 0x00020000);
  const unsigned wbase = mem * 131072u + (grp & 3) * 32768u;
  unsigned tag;

  if (__builtin_expect(wave < 8, 1)) {
    const int voff = lane * 16;
    const unsigned sbase = wbase + wave * 4096;
    auto offs = [&](int s) -> unsigned { return sbase + (((s + RG * g) & 15) << 20); };
    u32x4 kb[NBACK + 1][4];
#pragma unroll
    for (int s = 0; s <= NBACK; ++s) {
      const unsigned o = offs(s);
#pragma unroll
      for (int u = 0; u < 4; ++u) {
        if (s * 4 + u == BARF) {
          asm volatile("s_barrier" ::: "memory"); SCHED();
        }
        if (s * 4 + u >= IB) asm volatile("s_waitcnt vmcnt(%0)" :: "n"(PD - 1) : "memory");
        kb[s][u] = __builtin_amdgcn_raw_buffer_load_b128(rk, voff + u * 1024, o, KV_AUX);
        SCHED();
      }
    }
    {
      int spin = 0;
#pragma unroll 1
      while (__builtin_amdgcn_readfirstlane(LDL(&ctl[1])) == 0 && ++spin < SPIN_MAX) __builtin_amdgcn_s_sleep(1);
    }
    asm volatile("" ::: "memory");
    const u32x4 q0 = *(const u32x4*)&qs[(lane & 31) * 4], q1 = *(const u32x4*)&qs[128 + (lane & 31) * 4];
    tag = __builtin_amdgcn_readfirstlane(LDL(&ctl[2]));
    float mx = -INFINITY;
    const bool b3 = (lane & 8) != 0, b2 = (lane & 4) != 0;
    unsigned short* scw = &scs[wave * 16 + (lane >> 5) * 8 + ((lane >> 3) & 1) * 2 + (lane & 4) + ((lane >> 4) & 1)];
#pragma unroll
    for (int s = 0; s < NL; ++s) {
      kstep<true>(kb[NBACK], q0, q1, mx, scw + (NBACK + s) * 128, rk, voff, offs(NBACK + 1 + s), b3, b2);
#pragma unroll
      for (int i = s * BPL; i < s * BPL + BPL && i < NBACK; ++i) kstep<false>(kb[i], q0, q1, mx, scw + i * 128, rk, voff, 0, b3, b2);
      SCHED();
    }
    if (SE <= NBACK + NL) arrive_only(&ctl[7]);
#pragma unroll 1
    for (int n = NBACK + NL; n < 16; ++n) {
      const rsrc_t rn = __builtin_amdgcn_make_buffer_rsrc((void*)((n < 15 ? K : V) + (size_t)g * HEADB), 0, HEADB, 0x00020000);
      kstep<true>(kb[NBACK], q0, q1, mx, scw + n * 128, rn, voff, offs(n < 15 ? n + 1 : SE), b3, b2);
      if (n == SE - 1 || n == 15) arrive_only(&ctl[n == 15 ? 8 : 7]);
    }
    mx = __builtin_fmaxf(mx, dpp<XOR7>(mx)); mx = __builtin_fmaxf(mx, dpp<ROR8>(mx));
    mx = rbf(sw32_max(mx)) * CSC;
    float mcl = (lane & 1) ? rdl(mx, 16) : rdl(mx, 0);
    f32x2 a0[4], a1[4];
#pragma unroll
    for (int k = 0; k < 4; ++k) { a0[k] = f32x2{0.f, 0.f}; a1[k] = f32x2{0.f, 0.f}; }
    const int svo = (lane >> 5) * 8 + (lane & 7);
    const unsigned short* scr = &scs[wave * 16 + svo];
    unsigned sraw = scr[SE * 128];
#pragma unroll 1
    for (int n = SE + 1; n < 16; ++n) {
      const unsigned sn = scr[n * 128];
      vstep(kb[NBACK], sraw, mcl, a0, a1, rv, voff, offs(n));
      sraw = sn;
    }
    auto ticket = [&]() __attribute__((always_inline)) -> unsigned {
      asm volatile("s_waitcnt vmcnt(3)" ::: "memory");
      int t = arrive(&ctl[0]);
      asm volatile("s_waitcnt lgkmcnt(0)" : "+v"(t) :: "memory");
      t = __builtin_amdgcn_readfirstlane(t);
      unsigned o = __builtin_amdgcn_readfirstlane(LDL(&uaddr[t]));
      int spin = 0;
#pragma unroll 1
      while (o == EMPTY && ++spin < SPIN_MAX) { __builtin_amdgcn_s_sleep(1); o = __builtin_amdgcn_readfirstlane(LDL(&uaddr[t])); }
      asm volatile("" ::: "memory");
      return o == EMPTY ? OOB : o;
    };
    unsigned o = ticket();
    {
      const float gl = gm[lane & 1];
      const float f = __builtin_amdgcn_exp2f(mcl - gl);
      const float f0 = rdl(f, 0), f1 = rdl(f, 1);
      mcl = gl;
#pragma unroll
      for (int k = 0; k < 4; ++k) { a0[k] *= f0; a1[k] *= f1; }
    }
    const rsrc_t rs = __builtin_amdgcn_make_buffer_rsrc((void*)(ws + WS_SC + g * 131072), 0, 131072, 0x00020000);
#pragma unroll 1
    for (;;) {
      const unsigned sn = __builtin_amdgcn_raw_buffer_load_b16(rs, svo * 2, o >> 7, DEV);
      SCHED();
      vstep(kb[NBACK], sraw, mcl, a0, a1, rv, voff, o);
      if (o == OOB) break;
      sraw = sn;
      o = ticket();
    }
    f32x4 lo, hi;
    lo[0] = sw32_add(a0[0][0], a1[0][0]); lo[1] = sw32_add(a0[0][1], a1[0][1]);
    lo[2] = sw32_add(a0[1][0], a1[1][0]); lo[3] = sw32_add(a0[1][1], a1[1][1]);
    hi[0] = sw32_add(a0[2][0], a1[2][0]); hi[1] = sw32_add(a0[2][1], a1[2][1]);
    hi[2] = sw32_add(a0[3][0], a1[3][0]); hi[3] = sw32_add(a0[3][1], a1[3][1]);
    *(f32x4*)&macc[wave * 512 + lane * 4] = lo;
    *(f32x4*)&macc[wave * 512 + 256 + lane * 4] = hi;
    int arr = arrive(&ctl[3]);
    asm volatile("s_waitcnt lgkmcnt(0)" : "+v"(arr) :: "memory");
    if (__builtin_amdgcn_readfirstlane(arr) == 7) {
      f32x4 pl[7], ph[7];
#pragma unroll
      for (int w = 0; w < 7; ++w) { const int x = ((wave + 1 + w) & 7) * 512 + lane * 4; pl[w] = *(const f32x4*)&macc[x]; ph[w] = *(const f32x4*)&macc[x + 256]; }
      lo += pl[0] + (pl[1] + pl[2]) + ((pl[3] + pl[4]) + (pl[5] + pl[6]));
      hi += ph[0] + (ph[1] + ph[2]) + ((ph[3] + ph[4]) + (ph[5] + ph[6]));
      const float sv[8] = {lo[0], lo[1], lo[2], lo[3], hi[0], hi[1], hi[2], hi[3]};
      const int vo = (lane >> 3) * 16384 + (lane & 7) * 16;
#pragma unroll
      for (int i = 0; i < 4; ++i)
        put(rw, WS_H1 + g * 131072 + i * 4096 + j32 * 128 + (vo), f2u(sv[2 * i]), f2u(sv[2 * i + 1]), tag);
      SCHED();
      if (lane == 0) STL(&ctl[6], 1);
    }
    const int c2 = b / 9;
    if (b % 9 == 0 && c2 < 28) {
      const int ow = (c2 >> 2) * 32 + (lane & 7) * 4 + (c2 & 3);
      const unsigned xv = x32[ow];
      unsigned off[4]; float v0[4], v1[4];
#pragma unroll
      for (int i = 0; i < 4; ++i) { off[i] = WS_H2 + (c2 * 256 + wave * 32) * 128 + i * 1024 + lane * 16; v0[i] = 0.f; v1[i] = 0.f; }
      {
        int spin = 0;
#pragma unroll 1
        while (__builtin_amdgcn_readfirstlane(LDL(&ctl[6])) == 0 && ++spin < SPIN_MAX) __builtin_amdgcn_s_sleep(1);
        asm volatile("" ::: "memory");
        u32x4 t[4];
        poll_issue<4>(rw, off, t);
        poll_check<4>(t, off, tag, v0, v1);
#pragma unroll 1
        while (__builtin_amdgcn_readfirstlane(LDL(&ctl[5])) == 0 && ++spin < SPIN_MAX) __builtin_amdgcn_s_sleep(1);
        asm volatile("" ::: "memory");
      }
      gather<4>(rw, off, tag, v0, v1);
      float s = sw32_add(v0[0] + v0[1] + (v0[2] + v0[3]), v1[0] + v1[1] + (v1[2] + v1[3]));
      s += dpp<ROR8>(s); s = sw16_add(s, s);
      if ((lane & 24) == 0) red[wave * 16 + (lane & 7) * 2 + (lane >> 5)] = s;
      int fin = arrive(&ctl[4]);
      const f32x2 r = *(const f32x2*)&red[lane * 2];
      asm volatile("s_waitcnt lgkmcnt(0)" : "+v"(fin) :: "memory");
      if (__builtin_amdgcn_readfirstlane(fin) == 7) {
        float r0 = r[0], r1 = r[1];
        r0 += dpp<ROR8>(r0); r1 += dpp<ROR8>(r1);
        r0 = sw16_add(r0, r0); r1 = sw16_add(r1, r1);
        r0 = sw32_add(r0, r0); r1 = sw32_add(r1, r1);
        if (lane < 8) {
          const unsigned ov = pkbf(r0 + u2f(xv << 16), r1 + u2f(xv & 0xffff0000u));
          __builtin_amdgcn_raw_buffer_store_b32(ov, __builtin_amdgcn_make_buffer_rsrc((void*)out32, 0, 896, 0x00020000), ow * 4, 0, DEV);
        }
        if (b == 0 && lane == 0) __builtin_amdgcn_raw_buffer_store_b32(tag, rw, 0, WS_EPOCH, DEV);
      }
    }
    asm volatile("" :: "v"(kb[NBACK][0]), "v"(kb[NBACK][1]), "v"(kb[NBACK][2]), "v"(kb[NBACK][3]));
  } else {
    __builtin_amdgcn_s_setprio(3);
    const unsigned ep = __builtin_amdgcn_raw_buffer_load_b32(rw, 0, WS_EPOCH, DEV);
    unsigned xr[7];
#pragma unroll
    for (int j = 0; j < 7; ++j) xr[j] = x32[(lane >> 1) + 32 * j];
    u32x4 w[14];
    const int jq = mem * 4 + (grp & 3);
    {
      const rsrc_t r = __builtin_amdgcn_make_buffer_rsrc((void*)wq, 0, 448 * 8192, 0x00020000);
      const int vo = (lane >> 1) * 16384 + (lane & 1) * 16;
#pragma unroll
      for (int i = 0; i < 14; ++i) w[i] = __builtin_amdgcn_raw_buffer_load_b128(r, vo, g * 1024 + jq * 32 + (i >> 1) * 524288 + (i & 1) * 8192, KV_AUX);
    }
    if (j32 == 0 && lane == 0) __builtin_amdgcn_raw_buffer_store_b32(0u, rw, 0, WS_CTR + g * 256, DEV);
    SCHED();
    *(u32x4*)&uaddr[lane * 4] = u32x4{EMPTY, EMPTY, EMPTY, EMPTY};
    *(u32x4*)&uaddr[256 + lane * 4] = u32x4{EMPTY, EMPTY, EMPTY, EMPTY};
    if (lane < 16) ctl[lane] = 0;
    if (ABAR == 0) asm volatile("s_waitcnt lgkmcnt(0)\n s_barrier" ::: "memory");
    asm volatile("" : "+v"(xr[0]), "+v"(xr[1]), "+v"(xr[2]), "+v"(xr[3]), "+v"(xr[4]), "+v"(xr[5]), "+v"(xr[6]));
    unsigned xp[7];
    {
      float s = 0.f;
#pragma unroll
      for (int j = 0; j < 7; ++j) s += u2f(xr[j] << 16) + u2f(xr[j] & 0xffff0000u);
      const float mean = wave_sum(s) * (1.f / 896.f);
      float v = 0.f;
#pragma unroll
      for (int j = 0; j < 7; ++j) { const float da = u2f(xr[j] << 16) - mean, db = u2f(xr[j] & 0xffff0000u) - mean; v += da * da + db * db; }
      const float var = wave_sum(v) * (1.f / 896.f);
      const float mb = rbf(mean), rden = __builtin_amdgcn_rcpf(rbf(__builtin_amdgcn_sqrtf(rbf(rbf(var) + 1e-5f))));
#pragma unroll
      for (int j = 0; j < 7; ++j) xp[j] = pkbf(rbf(u2f(xr[j] << 16) - mb) * rden, rbf(u2f(xr[j] & 0xffff0000u) - mb) * rden);
    }
    tag = __builtin_amdgcn_readfirstlane(ep) + 1u;
    {
      float r0, r1;
      gemv16(w, xp, r0, r1);
      const unsigned pk = pkbf(r0, r1);
      auto s = __builtin_amdgcn_permlane16_swap(pk, pk, false, false);
      unsigned s0 = s[0], s1 = s[1];
      asm volatile("s_waitcnt vmcnt(0)" : "+v"(s0), "+v"(s1) :: "memory");
      if ((lane & 30) == 0)
        put(rw, WS_Q + g * 2048 + jq * 64 + (((lane & 1) * 2 + (lane >> 5)) * 16), s0, s1, tag);
    }
#pragma unroll
    for (int j = 0; j < 7; ++j) xs[(lane >> 1) + 32 * j] = xp[j];
    if (ABAR == 1) asm volatile("s_waitcnt lgkmcnt(0)\n s_barrier" ::: "memory");
    {
      unsigned off[2] = {WS_Q + g * 2048 + lane * 16u, WS_Q + g * 2048 + 1024 + lane * 16u};
      float v0[2] = {0.f, 0.f}, v1[2] = {0.f, 0.f};
      gather<2>(rw, off, tag, v0, v1);
      *(f32x2*)&qs[lane * 2] = f32x2{v0[0], v1[0]};
      *(f32x2*)&qs[128 + lane * 2] = f32x2{v0[1], v1[1]};
      if (lane == 0) STL(&ctl[2], (int)tag);
      asm volatile("s_waitcnt lgkmcnt(0)" ::: "memory");
      if (lane == 0) STL(&ctl[1], 1);
    }
    const int fu = grp * 7 + mem;
    if (mem < 7) {
      const rsrc_t rg = __builtin_amdgcn_make_buffer_rsrc((void*)wg, 0, 448 * 3584, 0x00020000);
      const rsrc_t ru = __builtin_amdgcn_make_buffer_rsrc((void*)wu, 0, 448 * 3584, 0x00020000);
      const int vo = (lane >> 3) * 3584 + (lane & 7) * 16, so = (fu & 7) * 56 * 3584 + (fu >> 3) * 128;
#pragma unroll
      for (int i = 0; i < 7; ++i) {
        w[2 * i] = __builtin_amdgcn_raw_buffer_load_b128(rg, vo, so + i * 8 * 3584, KV_AUX);
        w[2 * i + 1] = __builtin_amdgcn_raw_buffer_load_b128(ru, vo, so + i * 8 * 3584, KV_AUX);
      }
      float a[16];
#pragma unroll
      for (int i = 0; i < 16; ++i) a[i] = 0.f;
#pragma unroll
      for (int i = 0; i < 7; ++i) {
        const unsigned xl = ((const unsigned short*)xs)[(fu & 7) * 56 + i * 8 + (lane >> 3)], xh = xl << 16;
#pragma unroll
        for (int e = 0; e < 4; ++e) {
          const unsigned wa = w[2 * i][e], wb = w[2 * i + 1][e];
          a[2 * e] = dot2(wa, xl, a[2 * e]); a[2 * e + 1] = dot2(wa, xh, a[2 * e + 1]);
          a[8 + 2 * e] = dot2(wb, xl, a[8 + 2 * e]); a[8 + 2 * e + 1] = dot2(wb, xh, a[8 + 2 * e + 1]);
        }
      }
#pragma unroll
      for (int i = 0; i < 8; ++i) a[i] = sw32_add(a[i], a[i + 8]);
#pragma unroll
      for (int i = 0; i < 4; ++i) { a[i] = sw16_add(a[i], a[i + 4]); a[i] += dpp<ROR8>(a[i]); }
      const bool b3 = (lane & 8) != 0;
      const int src = (((lane & 7) << 3) | (lane >> 3)) * 4;
      const unsigned d0 = __builtin_amdgcn_ds_bpermute(src, f2u(b3 ? a[2] : a[0])), d1 = __builtin_amdgcn_ds_bpermute(src, f2u(b3 ? a[3] : a[1]));
      put(rw, WS_F + (fu >> 3) * 8192 + (fu & 7) * 128 + ((lane >> 3) * 1024 + (lane & 7) * 16), d0, d1, tag);
    }
    SCHED();
    float fo[8];
    unsigned wp[8][8], hp = 0;
    const rsrc_t ro = __builtin_amdgcn_make_buffer_rsrc((void*)wo, 0, 4096 * 896, 0x00020000);
    const rsrc_t rd = __builtin_amdgcn_make_buffer_rsrc((void*)wd, 0, 1792 * 896, 0x00020000);
    const int row0 = g * 512 + (j32 >> 2) * 64 + (j32 & 3) * 2;
    const unsigned wvo = lane < 56 ? lane * 16u : OOB;
    u32x4 ov[16], dv[8];
    auto issue_w = [&]() __attribute__((always_inline)) {
#pragma unroll
      for (int i = 0; i < 8; ++i) dv[i] = __builtin_amdgcn_raw_buffer_load_b128(rd, wvo, mem < 7 ? (fu * 8 + i) * 896 : OOB, KV_AUX);
#pragma unroll
      for (int i = 0; i < 16; ++i) ov[i] = __builtin_amdgcn_raw_buffer_load_b128(ro, wvo, (row0 + (i >> 1) * 8 + (i & 1)) * 896, KV_AUX);
      SCHED();
    };
    auto prep_d = [&]() __attribute__((always_inline)) {
#pragma unroll
      for (int i = 0; i < 8; ++i) fo[i] = 0.f;
#pragma unroll
      for (int pc = 0; pc < 4; ++pc) {
        const unsigned hs = __builtin_amdgcn_readlane(hp, pc);
#pragma unroll
        for (int k = 0; k < 4; ++k) {
          unsigned w0 = dv[2 * pc][k], w1 = dv[2 * pc + 1][k];
          fo[2 * k] = dot2(__builtin_amdgcn_perm(w1, w0, 0x05040100u), hs, fo[2 * k]);
          fo[2 * k + 1] = dot2(__builtin_amdgcn_perm(w1, w0, 0x07060302u), hs, fo[2 * k + 1]);
        }
      }
      asm volatile("" : "+v"(fo[0]), "+v"(fo[1]), "+v"(fo[2]), "+v"(fo[3]), "+v"(fo[4]), "+v"(fo[5]), "+v"(fo[6]), "+v"(fo[7]));
      SCHED();
    };
    auto prep_o = [&]() __attribute__((always_inline)) {
#pragma unroll
      for (int pc = 0; pc < 8; ++pc)
#pragma unroll
        for (int k = 0; k < 4; ++k) {
          unsigned w0 = ov[2 * pc][k], w1 = ov[2 * pc + 1][k];
          wp[pc][2 * k] = __builtin_amdgcn_perm(w1, w0, 0x05040100u);
          wp[pc][2 * k + 1] = __builtin_amdgcn_perm(w1, w0, 0x07060302u);
          asm volatile("" : "+v"(wp[pc][2 * k]), "+v"(wp[pc][2 * k + 1]));
        }
      SCHED();
    };
    {
      const unsigned t0 = __builtin_amdgcn_raw_buffer_load_b32(ro, 0, row0 * 896, KV_AUX), t1 = __builtin_amdgcn_raw_buffer_load_b32(rd, 0, fu * 8 * 896, KV_AUX);
      asm volatile("" :: "v"(t0), "v"(t1));
    }
    if (mem < 7) {
      unsigned off[1] = {WS_F + fu * 1024 + lane * 16u};
      float v0[1] = {0.f}, v1[1] = {0.f};
      gather<1>(rw, off, tag, v0, v1);
      float r0 = v0[0], r1 = v1[0];
      r0 += dpp<ROR8>(r0); r1 += dpp<ROR8>(r1);
      r0 = sw16_add(r0, r0); r1 = sw16_add(r1, r1);
      r0 = rbf(sw32_add(r0, r0)); r1 = rbf(sw32_add(r1, r1));
      const float u0 = dpp<ROR4>(r0), u1 = dpp<ROR4>(r1);
      const float s0 = rbf(r0 * __builtin_amdgcn_rcpf(1.f + __expf(-r0))), s1 = rbf(r1 * __builtin_amdgcn_rcpf(1.f + __expf(-r1)));
      hp = pkbf(s0 * u0, s1 * u1);
    }
    {
      int spin = 0;
#pragma unroll 1
      while (__builtin_amdgcn_readfirstlane(LDL(&ctl[7])) < 8 && ++spin < SPIN_MAX) __builtin_amdgcn_s_sleep(2);
      asm volatile("" ::: "memory");
      constexpr int NQ = (SE + 3) / 4;
      u32x4 sq[4];
      float m0 = -INFINITY, m1 = -INFINITY;
#pragma unroll
      for (int i = 0; i < NQ; ++i) {
        const int st = i * 4 + (lane >> 4) < SE ? i * 4 + (lane >> 4) : 0;
        sq[i] = *(const u32x4*)&scs[st * 128 + (lane & 15) * 8];
        __builtin_amdgcn_raw_buffer_store_b128(sq[i], rw, WS_SC + g * 131072 + (wbase >> 7) + (((st + RG * g) & 15) << 13) + (lane & 15) * 16, 0, DEV);
      }
      SCHED();
#pragma unroll
      for (int i = 0; i < 4 * NQ; ++i) { const unsigned w = sq[i >> 2][i & 3]; m0 = __builtin_fmaxf(m0, u2f(w << 16)); m1 = __builtin_fmaxf(m1, u2f(w & 0xffff0000u)); }
      m0 = wave_max(m0); m1 = wave_max(m1);
      asm volatile("s_waitcnt vmcnt(0)" : "+v"(m0), "+v"(m1) :: "memory");
      if (lane == 0) put(rw, WS_ML + g * 2048 + 1024 + j32 * 16, f2u(m0), f2u(m1), tag);
      unsigned off[1] = {WS_ML + g * 2048 + 1024 + (lane & 31) * 16u};
      float v0[1] = {0.f}, v1[1] = {0.f};
#pragma unroll 1
      while (__builtin_amdgcn_readfirstlane(LDL(&ctl[8])) < 8 && ++spin < SPIN_MAX) __builtin_amdgcn_s_sleep(2);
      asm volatile("" ::: "memory");
      gather<1, 4>(rw, off, tag, v0, v1);
      m0 = v0[0]; m1 = v1[0];
#pragma unroll
      for (int i = 0; i < 4; ++i) sq[i] = *(const u32x4*)&scs[i * 512 + lane * 8];
#pragma unroll
      for (int i = 0; i < 16; ++i) { const unsigned w = sq[i >> 2][i & 3]; m0 = __builtin_fmaxf(m0, u2f(w << 16)); m1 = __builtin_fmaxf(m1, u2f(w & 0xffff0000u)); }
      m0 = wave_max(m0) * CSC; m1 = wave_max(m1) * CSC;
      if (lane < 2) gm[lane] = lane ? m1 : m0;
      asm volatile("s_waitcnt lgkmcnt(0)" ::: "memory");
      if (lane < NPRE) STL(&uaddr[lane], (int)(wbase + (lane & 7) * 4096 + (((SE - TP + (lane >> 3) + RG * g) & 15) << 20)));
      float l0 = 0.f, l1 = 0.f;
#pragma unroll
      for (int i = 0; i < 16; ++i) {
        const unsigned w = sq[i >> 2][i & 3];
        l0 += __builtin_amdgcn_exp2f(__builtin_fmaf(u2f(w << 16), CSC, -m0)); l1 += __builtin_amdgcn_exp2f(__builtin_fmaf(u2f(w & 0xffff0000u), CSC, -m1));
      }
      l0 = wave_sum(l0); l1 = wave_sum(l1);
      if ((lane & 31) == 0)
        put(rw, WS_ML + g * 2048 + j32 * 16 + ((lane >> 5) * 512), f2u(lane ? m1 : m0), f2u(lane ? l1 : l0), tag);
    }
    {
      const rsrc_t rc = __builtin_amdgcn_make_buffer_rsrc((void*)(ws + WS_CTR + g * 256), 0, 4, 0x00020000);
      int k = NPRE, want = G1;
#pragma unroll 1
      for (int spin = 0; spin < 16 * SPIN_MAX; ++spin) {
        const int t = __builtin_amdgcn_readfirstlane(LDL(&ctl[0]));
        if (k - t <= THRESH) {
          const int gr = want > G1 ? G1 : k - t <= 0 && want < GMIN ? GMIN : want;
          const unsigned p = (unsigned)__builtin_amdgcn_readfirstlane(__builtin_amdgcn_raw_ptr_buffer_atomic_add_i32(gr, rc, lane * 4, 0, DEV));
          const int rem = NP - (int)p - gr;
          const bool last = rem <= 0 || k + gr + G1 > UCAP;
          const unsigned pu = p + lane;
          const unsigned o = (pu * 4096u + ((RG * g) << 20)) & (HEADB - 1);
          const bool valid = pu < (unsigned)NP && lane < gr;
          if (valid || last) STL(&uaddr[k + lane], (int)(valid ? o : OOB));
          if (last) break;
          k += gr;
          const int t2 = __builtin_amdgcn_readfirstlane(LDL(&ctl[0]));
          const float ta = 0.5f * (float)(t + t2);
          const float rho = (S_TICK * 8 + ta) * __builtin_amdgcn_rcpf(256.f - Q0 - rem * (1.f / 32.f));
          const int kfin = __builtin_amdgcn_readfirstlane((int)(rho * (rem * (1.f / 32.f) + Q0 + FL) - FL + ta + 0.5f));
          want = kfin - k < 0 ? 0 : kfin - k;
        } else {
          __builtin_amdgcn_s_sleep(1);
        }
      }
    }
    {
      unsigned off[4]; float v0[4], v1[4];
#pragma unroll
      for (int i = 0; i < 4; ++i) { off[i] = WS_H1 + b * 4096 + i * 1024 + lane * 16; v0[i] = 0.f; v1[i] = 0.f; }
      int spin = 0;
#pragma unroll 1
      while (__builtin_amdgcn_readfirstlane(LDL(&ctl[3])) < LATEW && ++spin < SPIN_MAX) __builtin_amdgcn_s_sleep(1);
      asm volatile("" ::: "memory");
      float f0, f1, f2, f3;
      {
        unsigned fo[1] = {WS_ML + g * 2048 + (j32 >> 4) * 512 + ((lane >> 3) + 8 * (lane & 3)) * 16u};
        float m[1] = {0.f}, l[1] = {0.f};
        u32x4 t[1];
        poll_issue<1>(rw, fo, t);
        issue_w();
        if (!poll_check<1>(t, fo, tag, m, l)) gather<1>(rw, fo, tag, m, l);
        float M = m[0];
        M = __builtin_fmaxf(M, dpp<XOR1>(M)); M = __builtin_fmaxf(M, dpp<XOR2>(M)); M = __builtin_fmaxf(M, dpp<ROR8>(M));
        M = sw16_max(M); M = sw32_max(M);
        float f = __builtin_amdgcn_exp2f(m[0] - M);
        float L = f * l[0];
        L += dpp<XOR1>(L); L += dpp<XOR2>(L); L += dpp<ROR8>(L); L = sw16_add(L, L); L = sw32_add(L, L);
        f *= __builtin_amdgcn_rcpf(L);
        f0 = dpp<0x00>(f); f1 = dpp<0x55>(f); f2 = dpp<0xAA>(f); f3 = dpp<0xFF>(f);
        asm volatile("" : "+v"(f0), "+v"(f1), "+v"(f2), "+v"(f3));
      }
      prep_d();
      prep_o();
#pragma unroll 1
      while (__builtin_amdgcn_readfirstlane(LDL(&ctl[6])) == 0 && ++spin < SPIN_MAX) __builtin_amdgcn_s_sleep(1);
      asm volatile("" ::: "memory");
      gather<4>(rw, off, tag, v0, v1);
      float s0 = f0 * v0[0] + f1 * v0[1] + (f2 * v0[2] + f3 * v0[3]), s1 = f0 * v1[0] + f1 * v1[1] + (f2 * v1[2] + f3 * v1[3]);
      s0 += dpp<ROR8>(s0); s1 += dpp<ROR8>(s1);
      s0 = sw16_add(s0, s0); s1 = sw16_add(s1, s1);
      s0 = sw32_add(s0, s0); s1 = sw32_add(s1, s1);
      const unsigned op = pkbf(s0, s1);
#pragma unroll
      for (int pc = 0; pc < 8; ++pc) {
        const unsigned os = __builtin_amdgcn_readlane(op, pc);
#pragma unroll
        for (int c = 0; c < 8; ++c) fo[c] = dot2(wp[pc][c], os, fo[c]);
      }
      if (lane < 56) {
        const int vo = (lane >> 3) * (4 * 256 * 128) + (lane & 7) * 16;
#pragma unroll
        for (int i = 0; i < 4; ++i)
          put(rw, WS_H2 + (i * 256 + b) * 128 + (vo), f2u(fo[2 * i]), f2u(fo[2 * i + 1]), tag);
      }
      SCHED();
      if (lane == 0) STL(&ctl[5], 1);
    }
  }
}

extern "C" void kernel_launch(void* const* d_in, const int* in_sizes, int n_in, void* d_out, int out_size, void* d_ws,
                              size_t ws_size, hipStream_t stream) {
  fused_decode<<<256, 576, 0, stream>>>((const unsigned*)d_in[0], (const char*)d_in[1], (const char*)d_in[2],
                                        (const char*)d_in[3], (const char*)d_in[4], (const char*)d_in[6],
                                        (const char*)d_in[7], (const char*)d_in[8], (unsigned*)d_out, (char*)d_ws);
}
```
